```python
import torch

def _softmax(x, axis=-1):
    return torch.softmax(x, dim=axis)
def _gelu(x):
    return torch.nn.functional.gelu(x, approximate="tanh")
def _silu(x):
    return torch.nn.functional.silu(x)
def _sigmoid(x):
    return torch.sigmoid(x)
def _layernorm(x, axis=-1, eps=1e-5):
    m = x.mean(dim=axis, keepdim=True)
    v = x.var(dim=axis, keepdim=True, unbiased=False)
    return (x - m) / torch.sqrt(v + eps)
def _scatter_add(tgt, idx, src, axis=0):
    return tgt.clone().index_add_(axis, idx, src)
def _segment_sum(src, seg, n):
    out = torch.zeros((n,) + src.shape[1:], dtype=src.dtype)
    return out.index_add_(0, seg, src)
def _pad(x, pw):
    flat = tuple(v for pair in reversed(pw) for v in pair)
    return torch.nn.functional.pad(x, flat)

kernel_name = 'concat_heads__linear_kernel_hedgehog__ssm_conv_s5__prompt'

def setup_inputs():

    _w = (lambda a: (lambda t: t.to(torch.bfloat16) if t.is_floating_point() else t)(torch.as_tensor(a)))
    def _A_setup():
        B, S, D, H = 1, 2048, 640, 32
        Dh = D // H
        return {
            "x": _w((lambda *s: torch.randn(*s, dtype=torch.bfloat16) * 0.4)(B, S, D)),
            "wqkv": _w((lambda *s: torch.randn(*s, dtype=torch.bfloat16) * 0.4)(D, 3 * D)),
            "w_phi": _w((lambda *s: torch.randn(*s, dtype=torch.bfloat16) * 0.4)(H, Dh, Dh) * (Dh ** -0.5)),
            "wo": _w((lambda *s: torch.randn(*s, dtype=torch.bfloat16) * 0.05)(D, D)),
        }
    _A_in = _A_setup()

    def _B_setup():
        B, S, D = 1, 2048, 640
        P = D
        Lambda_re = -0.5 * (lambda _x: torch.exp(torch.as_tensor(_x)))((lambda *s: torch.randn(*s, dtype=torch.bfloat16) * 0.8)(P))
        Lambda_im = (lambda *s: torch.randn(*s, dtype=torch.bfloat16) * 0.8)(P) * 1.0
        B_re = (lambda *s: torch.randn(*s, dtype=torch.bfloat16) * 0.8)(P, D) * (D ** -0.5)
        B_im = (lambda *s: torch.randn(*s, dtype=torch.bfloat16) * 0.8)(P, D) * (D ** -0.5)
        C_re = (lambda *s: torch.randn(*s, dtype=torch.bfloat16) * 0.8)(D, P) * (P ** -0.5)
        C_im = (lambda *s: torch.randn(*s, dtype=torch.bfloat16) * 0.8)(D, P) * (P ** -0.5)
        log_dt = (lambda *s: torch.randn(*s, dtype=torch.bfloat16) * 0.8)(P) * 0.3 - 2.0
        Dskip = (lambda *s: torch.randn(*s, dtype=torch.bfloat16) * 0.8)(D)
        return {
            "x": _w((lambda *s: torch.randn(*s, dtype=torch.bfloat16) * 0.05)(B, S, D)),
            "Lambda_re": _w(Lambda_re),
            "Lambda_im": _w(Lambda_im),
            "B_re": _w(B_re),
            "B_im": _w(B_im),
            "C_re": _w(C_re),
            "C_im": _w(C_im),
            "log_dt": _w(log_dt),
            "Dskip": _w(Dskip),
        }
    _B_in = _B_setup()

    return {
        "A_x": _A_in["x"],
        "A_wqkv": _A_in["wqkv"],
        "A_w_phi": _A_in["w_phi"],
        "A_wo": _A_in["wo"],
        "B_x": _B_in["x"],
        "B_Lambda_re": _B_in["Lambda_re"],
        "B_Lambda_im": _B_in["Lambda_im"],
        "B_B_re": _B_in["B_re"],
        "B_B_im": _B_in["B_im"],
        "B_C_re": _B_in["C_re"],
        "B_C_im": _B_in["C_im"],
        "B_log_dt": _B_in["log_dt"],
        "B_Dskip": _B_in["Dskip"],
    }

def reference(A_x, A_wqkv, A_w_phi, A_wo, B_x, B_Lambda_re, B_Lambda_im, B_B_re, B_B_im, B_C_re, B_C_im, B_log_dt, B_Dskip):
    def _A_ref(x, wqkv, w_phi, wo):
        B, S, D = x.shape
        H = 32; Dh = D // H
        qkv = (lambda x, *d: x.permute(*d))((x @ wqkv).reshape(B, S, 3, H, Dh), 2, 0, 3, 1, 4)
        q, k, v = qkv[0], qkv[1], qkv[2]
        pq = torch.einsum("bhsd,hdf->bhsf", q, w_phi)
        pk = torch.einsum("bhsd,hdf->bhsf", k, w_phi)
        phi_q = torch.concatenate([_softmax(pq, axis=-1), _softmax(-pq, axis=-1)], axis=-1)
        phi_k = torch.concatenate([_softmax(pk, axis=-1), _softmax(-pk, axis=-1)], axis=-1)
        kv = torch.einsum("bhsf,bhsd->bhfd", phi_k, v)
        z  = torch.einsum("bhsf->bhf", phi_k)
        num = torch.einsum("bhqf,bhfd->bhqd", phi_q, kv)
        den = torch.einsum("bhqf,bhf->bhq", phi_q, z)[..., None] + 1e-6
        o = num / den
        return (lambda x, *d: x.permute(*d))(o, 0, 2, 1, 3).reshape(B, S, D) @ wo

    def _B_ref(x, Lambda_re, Lambda_im, B_re, B_im, C_re, C_im, log_dt, Dskip):
        Bsz, L, H = x.shape
        Lam = Lambda_re + 1j * Lambda_im
        Bmat = B_re + 1j * B_im
        Cmat = C_re + 1j * C_im
        dt = torch.exp(log_dt)
        Lam_bar = torch.exp(dt * Lam)
        B_bar = ((Lam_bar - 1.0) / Lam)[:, None] * Bmat
        Bu = torch.einsum("bld,pd->blp", x + 0j, B_bar)
        h = torch.zeros_like(Bu[:, 0, :])
        hs = []
        for t in range(L):
            h = Lam_bar[None, :] * h + Bu[:, t, :]
            hs.append(h)
        hseq = torch.stack(hs, 1)
        y = 2.0 * torch.einsum("blp,dp->bld", hseq, Cmat).real + x * Dskip[None, None, :]
        return y

    a = _A_ref(A_x, A_wqkv, A_w_phi, A_wo)
    b = _B_ref(B_x, B_Lambda_re, B_Lambda_im, B_B_re, B_B_im, B_C_re, B_C_im, B_log_dt, B_Dskip)
    return torch.concatenate([a, b], axis=-1)
```

```cpp
#include <hip/hip_runtime.h>
#include <hip/hip_bf16.h>
#include <stdint.h>

typedef unsigned short u16;
__device__ __forceinline__ float bf2f(u16 v){ return __uint_as_float(((unsigned)v)<<16); }
__device__ __forceinline__ u16 f2bf(float f){ unsigned u=__float_as_uint(f); u += 0x7fffu + ((u>>16)&1u); return (u16)(u>>16); }
__device__ __forceinline__ float rbf(float f){ return bf2f(f2bf(f)); }

constexpr int S=2048, D=640, H=32, DH=20;

__global__ void gemm_naive(const u16* A, const u16* B, float* C, int M, int N, int K, int bt, int ldc, int round){
  int n = blockIdx.x*blockDim.x+threadIdx.x; int m = blockIdx.y;
  if(n>=N) return;
  float acc=0;
  for(int k=0;k<K;k++) acc += bf2f(A[m*K+k])*bf2f(bt? B[n*K+k] : B[k*N+n]);
  C[m*ldc+n] = round? rbf(acc):acc;
}
__global__ __launch_bounds__(128) void phi_kernel(const float* qkv, const u16* wphi, float* phi){
  int idx = blockIdx.x*blockDim.x+threadIdx.x;
  if(idx>=2*S*H) return;
  int h = idx%H; int s=(idx/H)%S; int w=idx/(H*S);
  float p[DH];
  #pragma unroll
  for(int f=0;f<DH;f++){ float a=0; for(int d=0;d<DH;d++) a+= qkv[s*1920+w*640+h*DH+d]*bf2f(wphi[h*400+d*DH+f]); p[f]=rbf(a);}
  float mx=-1e30f,mn=1e30f;
  #pragma unroll
  for(int f=0;f<DH;f++){mx=fmaxf(mx,p[f]);mn=fminf(mn,p[f]);}
  float s1=0,s2=0;
  #pragma unroll
  for(int f=0;f<DH;f++){s1+=expf(p[f]-mx); s2+=expf(mn-p[f]);}
  float* o = phi + (size_t)idx*40;
  #pragma unroll
  for(int f=0;f<DH;f++){o[f]=rbf(expf(p[f]-mx)/s1); o[20+f]=rbf(expf(mn-p[f])/s2);}
}
__global__ void kv_kernel(const float* qkv, const float* phi, float* kv){
  int idx = blockIdx.x*blockDim.x+threadIdx.x;
  if(idx>=H*40*21) return;
  int d=idx%21, f=(idx/21)%40, h=idx/(21*40);
  float a=0;
  for(int s=0;s<S;s++){ float pk=phi[(((size_t)S+s)*H+h)*40+f]; float v = d<20? qkv[s*1920+1280+h*DH+d]:1.f; a+=pk*v; }
  kv[idx]=rbf(a);
}
__global__ void o_kernel(const float* phi, const float* kv, u16* o){
  int idx = blockIdx.x*blockDim.x+threadIdx.x;
  if(idx>=S*D) return;
  int d=idx%DH, h=(idx/DH)%H, s=idx/D;
  float n=0,z=0;
  for(int f=0;f<40;f++){ float pq=phi[((size_t)s*H+h)*40+f]; n+=pq*kv[(h*40+f)*21+d]; z+=pq*kv[(h*40+f)*21+20]; }
  n=rbf(n); z=rbf(rbf(z)+1e-6f);
  o[idx]=f2bf(n/z);
}
__global__ void scan_kernel(float* ur, float* ui, const u16* lre, const u16* lim, const u16* logdt){
  int p = blockIdx.x*blockDim.x+threadIdx.x; if(p>=D) return;
  float dt = rbf(expf(bf2f(logdt[p])));
  float a=bf2f(lre[p]), b=bf2f(lim[p]);
  float e=expf(dt*a); float lr=e*cosf(dt*b), li=e*sinf(dt*b);
  float nr=lr-1.f, ni=li; float den=a*a+b*b;
  float cr=(nr*a+ni*b)/den, ci=(ni*a-nr*b)/den;
  float hr=0,hi=0;
  for(int t=0;t<S;t++){
    float xr=ur[t*D+p], xi=ui[t*D+p];
    float br=cr*xr-ci*xi, bi=cr*xi+ci*xr;
    float nhr=lr*hr-li*hi+br, nhi=lr*hi+li*hr+bi; hr=nhr; hi=nhi;
    ur[t*D+p]=hr; ui[t*D+p]=hi;
  }
}
__global__ void y_kernel(const float* hr, const float* hi, const u16* cre, const u16* cim, const u16* x, const u16* dsk, float* out){
  int n = blockIdx.x*blockDim.x+threadIdx.x; int m = blockIdx.y;
  if(n>=D) return;
  float acc=0;
  for(int k=0;k<D;k++) acc += hr[m*D+k]*bf2f(cre[n*D+k]) - hi[m*D+k]*bf2f(cim[n*D+k]);
  out[m*1280+640+n] = 2.f*acc + rbf(bf2f(x[m*D+n])*bf2f(dsk[n]));
}

extern "C" void kernel_launch(void* const* d_in, const int* in_sizes, int n_in,
                              void* d_out, int out_size, void* d_ws, size_t ws_size,
                              hipStream_t stream) {
  const u16* Ax=(const u16*)d_in[0]; const u16* wqkv=(const u16*)d_in[1]; const u16* wphi=(const u16*)d_in[2]; const u16* wo=(const u16*)d_in[3];
  const u16* Bx=(const u16*)d_in[4]; const u16* lre=(const u16*)d_in[5]; const u16* lim=(const u16*)d_in[6];
  const u16* bre=(const u16*)d_in[7]; const u16* bim=(const u16*)d_in[8]; const u16* cre=(const u16*)d_in[9]; const u16* cim=(const u16*)d_in[10];
  const u16* logdt=(const u16*)d_in[11]; const u16* dsk=(const u16*)d_in[12];
  float* out=(float*)d_out;
  float* ws=(float*)d_ws;
  float* qkv=ws; float* phi=qkv+S*1920; float* kv=phi+2*S*H*40; float* ur=kv+H*40*21+64; float* ui=ur+S*D; u16* o=(u16*)(ui+S*D);
  gemm_naive<<<dim3(1920/128,S),128,0,stream>>>(Ax,wqkv,qkv,S,1920,D,0,1920,1);
  phi_kernel<<<2*S*H/128,128,0,stream>>>(qkv,wphi,phi);
  kv_kernel<<<(H*40*21+63)/64,64,0,stream>>>(qkv,phi,kv);
  o_kernel<<<S*D/128,128,0,stream>>>(phi,kv,o);
  gemm_naive<<<dim3(640/128,S),128,0,stream>>>(o,wo,out,S,640,D,0,1280,1);
  gemm_naive<<<dim3(640/128,S),128,0,stream>>>(Bx,bre,ur,S,640,D,1,640,0);
  gemm_naive<<<dim3(640/128,S),128,0,stream>>>(Bx,bim,ui,S,640,D,1,640,0);
  scan_kernel<<<10,64,0,stream>>>(ur,ui,lre,lim,logdt);
  y_kernel<<<dim3(640/128,S),128,0,stream>>>(ur,ui,cre,cim,Bx,dsk,out);
}
```

```cpp
#include <hip/hip_runtime.h>
#include <hip/hip_bf16.h>
#include <stdint.h>

typedef unsigned short u16;
typedef unsigned long long u64;
typedef __bf16 bf16x8 __attribute__((ext_vector_type(8)));
typedef __bf16 bf16x4 __attribute__((ext_vector_type(4)));
typedef __bf16 bf16x2 __attribute__((ext_vector_type(2)));
typedef float f32x4 __attribute__((ext_vector_type(4)));
typedef float f32x2 __attribute__((ext_vector_type(2)));
typedef unsigned u32x4 __attribute__((ext_vector_type(4)));
typedef unsigned u32x2 __attribute__((ext_vector_type(2)));
#define LAS __attribute__((address_space(3)))
#define GAS __attribute__((address_space(1)))
#define CAS __attribute__((address_space(4)))
#define DI __device__ __forceinline__
#define SCHED() __builtin_amdgcn_sched_barrier(0)
typedef float f32x16 __attribute__((ext_vector_type(16)));

constexpr unsigned WS_EPOCH_A = 0, WS_EPOCH_B = 256;
constexpr unsigned WS_FA1 = 4096;
constexpr unsigned WS_FA2 = WS_FA1 + 512 * 128;
constexpr unsigned WS_FB2 = WS_FA2 + 128 * 128;
constexpr unsigned WS_GB1 = WS_FB2 + 128 * 128;
constexpr unsigned WS_KVF = WS_GB1 + 128 * 1280;
constexpr unsigned WS_KVP = WS_KVF + 32 * 4096;
constexpr unsigned WS_OB = WS_KVP + 512 * 4096;
constexpr unsigned WS_HB = WS_OB + 1024 * 2560;
constexpr unsigned WS_END = WS_HB + 16 * 320 * 1024;
constexpr unsigned SPIN_CAP = 1u << 17;

struct Params {
  const u16 *Ax, *wqkv, *wphi, *wo, *Bx, *lre, *lim, *bre, *bim, *cre, *cim, *logdt, *dsk;
  float* out;
  char* ws;
};

DI float bf2f(unsigned v) { return __uint_as_float(v << 16); }
DI float rbf(float f) { return (float)(__bf16)f; }
DI unsigned pk2(float a, float b) { f32x2 f = {a, b}; return __builtin_bit_cast(unsigned, __builtin_convertvector(f, bf16x2)); }
DI u32x4 mksrd(const void* p, unsigned bytes) {
  const u64 a = (u64)p;
  u32x4 r = {(unsigned)a, (unsigned)(a >> 32) & 0xffffu, bytes, 0x00020000u};
  return r;
}
DI __amdgpu_buffer_rsrc_t mkrs(const void* p, unsigned bytes) { return __builtin_amdgcn_make_buffer_rsrc((void*)p, 0, bytes, 0x00020000); }
template <int LI, int SI>
DI void dma16(u32x4 srd, unsigned vo, unsigned so, unsigned lb) {
  unsigned t;
  asm volatile("s_add_u32 m0, %[lb], %[li]\n\ts_add_u32 %[t], %[so], %[si]\n\tbuffer_load_dwordx4 %[vo], %[srd], %[t] offen lds"
               : [t] "=&s"(t) : [lb] "s"(lb), [li] "i"(LI), [so] "s"(so), [si] "i"(SI), [vo] "v"(vo), [srd] "s"(srd) : "memory", "scc");
}
DI void kadv(unsigned& k) { asm volatile("s_add_u32 %0, %0, 0x80\n\ts_cmp_eq_u32 %0, 0x500\n\ts_cselect_b32 %0, 0, %0" : "+s"(k)::"scc"); }
DI void mfma32(f32x16& c, bf16x8 a, bf16x8 b) { asm volatile("v_mfma_f32_32x32x16_bf16 %0, %1, %2, %0" : "+v"(c) : "v"(a), "v"(b)); }
DI void mfma32z(f32x16& c, bf16x8 a, bf16x8 b) { asm volatile("v_mfma_f32_32x32x16_bf16 %0, %1, %2, 0" : "=&v"(c) : "v"(a), "v"(b)); }
DI void mfma16(f32x4& c, bf16x8 a, bf16x8 b) { asm volatile("v_mfma_f32_16x16x32_bf16 %0, %1, %2, %0" : "+v"(c) : "v"(a), "v"(b)); }
DI void swap16x4(u32x4& a, u32x4& b) {
#pragma unroll
  for (int k = 0; k < 4; ++k) {
    const auto r = __builtin_amdgcn_permlane16_swap(a[k], b[k], false, false);
    const unsigned r0 = r[0], r1 = r[1];
    a[k] = r0; b[k] = r1;
  }
}
DI void mfma16x2(f32x4& c0, f32x4& c1, u32x4 a0, u32x4 a1, bf16x8 b) {
  asm volatile("s_nop 1\n\tv_mfma_f32_16x16x32_bf16 %0, %2, %4, %0\n\tv_mfma_f32_16x16x32_bf16 %1, %3, %4, %1" : "+v"(c0), "+v"(c1) : "v"(a0), "v"(a1), "v"(b));
}
template <int N, int NW> struct LBase {
  unsigned v[NW][N];
  DI void fin() {
#pragma unroll
    for (int k = 1; k < NW; ++k)
#pragma unroll
      for (int n = 0; n < N; ++n) { v[k][n] = v[0][n] + k * 65536; asm volatile("" : "+v"(v[k][n])); }
  }
  template <int OFF> DI unsigned at(int n) const { return v[OFF >> 16][n] + (OFF & 65535); }
};
template <int N> DI void vmwait() { asm volatile("s_waitcnt vmcnt(%0)" ::"i"(N) : "memory"); }
DI void bar() { asm volatile("s_waitcnt lgkmcnt(0)\n\ts_barrier" ::: "memory"); }
DI float dppx(float x) { return __builtin_bit_cast(float, __builtin_amdgcn_update_dpp(0, __builtin_bit_cast(int, x), 0xB1, 0xF, 0xF, true)); }
DI bf16x8 ldsr128(LAS char* l, unsigned off) { return *(LAS bf16x8*)(l + off); }
DI bf16x4 ldstr(LAS char* l, unsigned off) { return __builtin_amdgcn_ds_read_tr16_b64_v4bf16((LAS bf16x4*)(l + off)); }
DI bf16x8 cat(bf16x4 a, bf16x4 b) { return __builtin_shufflevector(a, b, 0, 1, 2, 3, 4, 5, 6, 7); }
#define STAMP(k) do {} while (0)
#define PROF_ARG
#define PROF_PASS
#define PROF_FLUSH() do {} while (0)

DI void scan_step(float& o0, float& o1, float& o2, float& o3, float& o4, float u0, float u1, float u2, float u3, float u4,
                  const float (&s)[5], const float (&a)[5], const float (&b)[5]) {
  asm("v_fma_f32 %0, %15, %10, %5\n\tv_fma_f32 %1, %16, %11, %6\n\tv_fma_f32 %2, %17, %12, %7\n\tv_fma_f32 %3, %18, %13, %8\n\tv_fma_f32 %4, %19, %14, %9\n\t"
      "v_fmac_f32_dpp %0, %10, %20 quad_perm:[1,0,3,2] row_mask:0xf bank_mask:0xf\n\t"
      "v_fmac_f32_dpp %1, %11, %21 quad_perm:[1,0,3,2] row_mask:0xf bank_mask:0xf\n\t"
      "v_fmac_f32_dpp %2, %12, %22 quad_perm:[1,0,3,2] row_mask:0xf bank_mask:0xf\n\t"
      "v_fmac_f32_dpp %3, %13, %23 quad_perm:[1,0,3,2] row_mask:0xf bank_mask:0xf\n\t"
      "v_fmac_f32_dpp %4, %14, %24 quad_perm:[1,0,3,2] row_mask:0xf bank_mask:0xf"
      : "=&v"(o0), "=&v"(o1), "=&v"(o2), "=&v"(o3), "=&v"(o4)
      : "v"(u0), "v"(u1), "v"(u2), "v"(u3), "v"(u4), "v"(s[0]), "v"(s[1]), "v"(s[2]), "v"(s[3]), "v"(s[4]),
        "v"(a[0]), "v"(a[1]), "v"(a[2]), "v"(a[3]), "v"(a[4]), "v"(b[0]), "v"(b[1]), "v"(b[2]), "v"(b[3]), "v"(b[4]));
}

DI void poll_flags(const char* base, unsigned stride, int n, int lane, u64 tag) {
  const GAS u64* fp = (const GAS u64*)(base + (unsigned)(lane < n ? lane : 0) * stride);
  for (unsigned sp = 0; sp < SPIN_CAP; ++sp) {
    const u64 v = __hip_atomic_load(fp, __ATOMIC_RELAXED, __HIP_MEMORY_SCOPE_AGENT);
    if (__all(v == tag)) break;
    __builtin_amdgcn_s_sleep(1);
  }
  asm volatile("" ::: "memory");
}

constexpr int RING = 36864, TB = 110592, T_PAR = TB, T_POW = TB + 2560, T_E = TB + 7680, T_L = TB + 12800, T_H = 161792;

template <int KC, int U>
DI void b1_dma(u32x4 sX, u32x4 sW, unsigned vx0, unsigned vx1, unsigned vw, unsigned sox, unsigned sow, unsigned lbx, unsigned lbw) {
  constexpr int slot = (KC % 3) * RING;
  if constexpr (U < 4) dma16<slot + U * 1024, U * 4 * 1280>(sX, (U & 1) ? vx1 : vx0, sox, lbx);
  else dma16<slot + 16384 + (U - 4) * 1024, (U - 4) * 10240>(sW, vw, sow, lbw);
}
constexpr int cbase(int ci, int part) {
  return ci == 3 ? part * 10240 : ci == 4 ? (part ? 98304 : 20480) : (ci == 5 || ci == 6) ? TB + (ci - 5) * 20480 + part * 10240
                                : 36864 + (ci >= 7 ? ci - 7 : ci) * 20480 + part * 10240;
}

constexpr int late_dma(int ks, int slot) {
  int n = 0;
  for (int k = 11; k <= 21; ++k)
    for (int sl = 0; sl < 2; ++sl) {
      const bool on = sl == 0 || k == 12 || k == 14 || k == 16 || k == 18;
      if (k == ks && sl == slot) return on ? n : -1;
      if (on) ++n;
    }
  return -1;
}
struct Disc { float ld, a, b, dt, e, y, n, r, z, sp, cp, lr, li, cr, ci, t0, t1, t2, qr, qi, pr, pi; };
#define PIN1(x) asm volatile("" : "+v"(x))
#define PIN2(x, y) asm volatile("" : "+v"(x), "+v"(y))
#define PIN3(x, y, z) asm volatile("" : "+v"(x), "+v"(y), "+v"(z))
template <int K>
DI void disc_slice(Disc& D, LAS char* lds, int pl) {
  constexpr float L2E = 1.4426950408889634f;
  if constexpr (K == 0) { D.dt = rbf(__builtin_amdgcn_exp2f(D.ld * L2E)); PIN1(D.dt); }
  if constexpr (K == 1) { D.e = __builtin_amdgcn_exp2f((D.dt * D.a) * L2E); D.y = D.dt * D.b; PIN2(D.e, D.y); }
  if constexpr (K == 2) { D.n = __builtin_rintf(D.y * 0.6366197723675814f); D.r = __builtin_fmaf(D.n, -1.5703125f, D.y); PIN2(D.n, D.r); }
  if constexpr (K == 3) { D.r = __builtin_fmaf(D.n, -4.837512969970703125e-4f, D.r); D.r = __builtin_fmaf(D.n, -7.54978995489188216e-8f, D.r); D.z = D.r * D.r; PIN2(D.r, D.z); }
  if constexpr (K == 4) { D.sp = __builtin_fmaf(__builtin_fmaf(D.z, -1.9515295891e-4f, 8.3321608736e-3f), D.z, -1.6666654611e-1f); D.t0 = D.r * D.z; PIN2(D.sp, D.t0); }
  if constexpr (K == 5) { D.sp = __builtin_fmaf(D.sp, D.t0, D.r); D.cp = __builtin_fmaf(__builtin_fmaf(D.z, 2.443315711809948e-5f, -1.388731625493765e-3f), D.z, 4.166664568298827e-2f); PIN2(D.sp, D.cp); }
  if constexpr (K == 6) { D.t0 = D.z * D.z; D.t1 = __builtin_fmaf(D.z, -0.5f, 1.f); D.cp = __builtin_fmaf(D.cp, D.t0, D.t1); PIN1(D.cp); }
  if constexpr (K == 7) { const int q = (int)D.n; D.t2 = __int_as_float(q); D.t0 = (q & 1) ? D.cp : D.sp; D.t1 = (q & 1) ? D.sp : D.cp; PIN3(D.t0, D.t1, D.t2); }
  if constexpr (K == 8) { const unsigned q = __float_as_uint(D.t2); D.t0 = __uint_as_float(__float_as_uint(D.t0) ^ ((q & 2u) << 30)); D.t2 = __uint_as_float(((q + 1u) & 2u) << 30); PIN2(D.t0, D.t2); }
  if constexpr (K == 9) { D.t1 = __uint_as_float(__float_as_uint(D.t1) ^ __float_as_uint(D.t2)); D.li = D.e * D.t0; D.lr = D.e * D.t1; PIN2(D.lr, D.li); }
  if constexpr (K == 10) { D.t0 = __builtin_amdgcn_rcpf(fmaxf(fabsf(D.a), fabsf(D.b))); D.t2 = D.lr - 1.f; PIN2(D.t0, D.t2); }
  if constexpr (K == 11) { D.a *= D.t0; D.b *= D.t0; D.t1 = D.a * D.a; PIN3(D.a, D.b, D.t1); }
  if constexpr (K == 12) { D.t1 = __builtin_amdgcn_rcpf(__builtin_fmaf(D.b, D.b, D.t1)); D.cr = D.t2 * D.a; PIN2(D.t1, D.cr); }
  if constexpr (K == 13) { D.t1 *= D.t0; D.cr = __builtin_fmaf(D.li, D.b, D.cr); D.ci = D.li * D.a; PIN3(D.t1, D.cr, D.ci); }
  if constexpr (K == 14) { D.ci = __builtin_fmaf(-D.t2, D.b, D.ci) * D.t1; D.cr *= D.t1; PIN2(D.cr, D.ci); }
  if constexpr (K == 15) { *(LAS f32x4*)(lds + T_PAR + pl * 32) = (f32x4){D.lr, D.li, D.cr, D.ci}; D.qr = D.lr; D.qi = D.li; }
  if constexpr (K >= 16 && K < 20) { const float t = __builtin_fmaf(D.qr, D.qr, -D.qi * D.qi); D.qi = (D.qr + D.qr) * D.qi; D.qr = t; PIN2(D.qr, D.qi); }
  if constexpr (K == 20) { *(LAS f32x2*)(lds + T_POW + pl * 8) = (f32x2){1.f, 0.f}; D.pr = D.qr; D.pi = D.qi; }
  if constexpr (K >= 21 && K < 28) {
    *(LAS f32x2*)(lds + T_POW + ((K - 20) * 80 + pl) * 8) = (f32x2){D.pr, D.pi};
    const float t = __builtin_fmaf(D.pr, D.qr, -D.pi * D.qi); D.pi = __builtin_fmaf(D.pr, D.qi, D.pi * D.qr); D.pr = t; PIN2(D.pr, D.pi);
  }
  if constexpr (K == 28) *(LAS f32x4*)(lds + T_PAR + pl * 32 + 16) = (f32x4){D.pr, D.pi, D.qr, D.qi};
}

DI void chain_b(const Params& P, LAS char* lds, int c, int j, int rot, int w, int lane PROF_ARG) {
  const int g = lane >> 4, i = lane & 15, h = lane >> 5, i32 = lane & 31, e3 = lane >> 3, pos = lane & 7, wr = w >> 1, wc = w & 1;
  const u32x4 sX = mksrd(P.Bx, 2048 * 1280), sW = mksrd(wr ? P.bim : P.bre, 640 * 1280);
  const unsigned vxr = (16 * (e3 >> 2) + (e3 & 3)) * 1280;
  const unsigned vx0 = vxr + ((pos ^ (e3 >> 1)) << 4), vx1 = vxr + ((pos ^ (4 + (e3 >> 1))) << 4);
  const unsigned vw = e3 * 1280 + ((pos ^ ((e3 & 6) | wr)) << 4);
  const unsigned sx0 = (c * 128 + 32 * w) * 1280, sw0 = (j * 80 + 40 * wc) * 1280;
  const unsigned lbx = w * 4096, lbw = w * 5120;
  unsigned kc1 = rot * 128;
#define B1ISSUE(KC, U) do { b1_dma<KC, U>(sX, sW, vx0, vx1, vw, sx0 + kc1, sw0 + kc1, lbx, lbw); if ((U) == 8) kadv(kc1); } while (0)
#define B1ALL(KC) do { B1ISSUE(KC, 0); B1ISSUE(KC, 4); B1ISSUE(KC, 5); B1ISSUE(KC, 1); B1ISSUE(KC, 6); B1ISSUE(KC, 2); B1ISSUE(KC, 7); B1ISSUE(KC, 3); B1ISSUE(KC, 8); } while (0)
  const int plc = 20 * w + (lane < 20 ? lane : 0);
  unsigned in0, in1, in2;
  { u32x4 t_ = sX; asm volatile("" : "+s"(t_)); STAMP(1); }
  B1ISSUE(0, 0);
  STAMP(2);
  B1ISSUE(0, 4); B1ISSUE(0, 5); B1ISSUE(0, 1); B1ISSUE(0, 6);
  STAMP(3);
  B1ISSUE(0, 2); B1ISSUE(0, 7); B1ISSUE(0, 3); B1ISSUE(0, 8);
  STAMP(4);
  {
    const unsigned po = (j * 80 + plc) * 2;
    asm volatile("global_load_ushort %0, %3, %4\n\tglobal_load_ushort %1, %3, %5\n\tglobal_load_ushort %2, %3, %6"
                 : "=&v"(in0), "=&v"(in1), "=&v"(in2) : "v"(po), "s"(P.logdt), "s"(P.lre), "s"(P.lim) : "memory");
  }
  u64 tag = *(const CAS u64*)(P.ws + WS_EPOCH_B);
  const u32x4 sC = mksrd(wr ? P.cim : P.cre, 640 * 1280);
  unsigned vC[5];
#pragma unroll
  for (int u = 0; u < 5; ++u) {
    const int R = 8 * (5 * wc + u) + e3, d = R < 64 ? 2 * (R & 31) + (R >> 5) : R;
    vC[u] = (j * 80 + d) * 1280 + ((pos ^ ((R >> 1) & 7)) << 4);
  }
  const unsigned lb4 = (wr ? 98304 : 20480) + wc * 5120;
  unsigned kc2 = rot * 128;
#define B2ISSUE(CI, U) do { dma16<((CI) == 4 ? 0 : cbase(CI, 0)) + (U) * 1024, 0>(sC, vC[U], kc2, (CI) == 4 ? lb4 : lbw); if ((U) == 4) kadv(kc2); } while (0)
  f32x16 acc[5];
  const unsigned ra = (w * 32 + i32) * 128, rb = 16384 + ((i32 & 1) * 80 + (i32 >> 1)) * 128;
  const unsigned ma = (i32 >> 1) & 7, mb = (((i32 >> 2) & 3) << 1) | (i32 & 1);
  LBase<4, 2> aA, aB;
#pragma unroll
  for (int s = 0; s < 4; ++s) { aA.v[0][s] = ra + (((2 * s + h) ^ ma) << 4); aB.v[0][s] = rb + (((2 * s + h) ^ mb) << 4); }
  aA.fin(); aB.fin();
  bf16x8 fa[2][2], fb[2][2][5];
#define B1RD(KC, HF, Q) do { constexpr int sl = ((KC) % 3) * RING, s2 = (Q) / 6, q6 = (Q) % 6; \
    if (q6 == 0) fa[HF][s2] = ldsr128(lds, aA.at<sl>(2 * (HF) + s2)); else fb[HF][s2][q6 - 1] = ldsr128(lds, aB.at<sl + (q6 > 0 ? q6 - 1 : 0) * 2048>(2 * (HF) + s2)); } while (0)
  SCHED();
  B1ALL(1);
  STAMP(5);
  asm volatile("s_waitcnt vmcnt(9)" : "+v"(in0), "+v"(in1), "+v"(in2)::"memory");
  bar();
  STAMP(6);
  asm volatile("" : "+s"(tag));
  tag += 0x9E3779B97F4A7C15ull;
  const unsigned tagl = (unsigned)tag, tagh = (unsigned)(tag >> 32);
  Disc D; D.ld = bf2f(in0); D.a = bf2f(in1); D.b = bf2f(in2);
#define B1RD2(KC, HF, X) do { B1RD(KC, HF, 2 * (X)); B1RD(KC, HF, 2 * (X) + 1); } while (0)
  B1RD2(0, 0, 0); B1RD2(0, 0, 1); B1RD2(0, 0, 2); B1RD2(0, 0, 3); B1RD2(0, 0, 4); B1RD2(0, 0, 5);
  SCHED();
#define B1GAP(KC, HF, X, RKC, RD, ISS) do { \
    if ((KC) == 0 && (HF) == 0 && (X) < 5) mfma32z(acc[(X) % 5], fa[HF][(X) / 5], fb[HF][(X) / 5][(X) % 5]); \
    else mfma32(acc[(X) % 5], fa[HF][(X) / 5], fb[HF][(X) / 5][(X) % 5]); \
    if (RD) { if ((X) == 0) B1RD2(RKC, 1 - (HF), 0); if ((X) == 1) B1RD2(RKC, 1 - (HF), 1); if ((X) == 3) B1RD2(RKC, 1 - (HF), 2); \
              if ((X) == 4) B1RD2(RKC, 1 - (HF), 3); if ((X) == 6) B1RD2(RKC, 1 - (HF), 4); if ((X) == 7) B1RD2(RKC, 1 - (HF), 5); } \
    if ((KC) == 0 && (HF) == 0) { if ((X) >= 1) B1ISSUE(2, ((X) >= 1 ? (X) - 1 : 0)); } \
    else { if ((X) == 2) { ISS(0); } if ((X) == 5) { ISS(1); } if ((X) == 8) { ISS(2); } if ((X) == 9) { ISS(3); } if ((X) == 0) { ISS(4); } } \
    if ((KC) >= 1 && (KC) <= 3) { constexpr int ds_ = ((KC) - 1) * 12 + (HF) * 6 + ((X) == 0 ? 0 : (X) == 1 ? 1 : (X) == 3 ? 2 : (X) == 4 ? 3 : (X) == 6 ? 4 : (X) == 7 ? 5 : -100); \
      if constexpr (ds_ >= 0 && ds_ <= 28) disc_slice<ds_>(D, lds, plc); } \
    SCHED(); } while (0)
#define B1HALF(KC, HF, RKC, RD, ISS) do { B1GAP(KC, HF, 0, RKC, RD, ISS); B1GAP(KC, HF, 1, RKC, RD, ISS); B1GAP(KC, HF, 2, RKC, RD, ISS); B1GAP(KC, HF, 3, RKC, RD, ISS); \
    B1GAP(KC, HF, 4, RKC, RD, ISS); B1GAP(KC, HF, 5, RKC, RD, ISS); B1GAP(KC, HF, 6, RKC, RD, ISS); B1GAP(KC, HF, 7, RKC, RD, ISS); B1GAP(KC, HF, 8, RKC, RD, ISS); B1GAP(KC, HF, 9, RKC, RD, ISS); } while (0)
#define B1STEP(KC, ISSA, VM, ISSB) do { B1HALF(KC, 0, KC, true, ISSA); if ((KC) < 9) { vmwait<VM>(); bar(); } B1HALF(KC, 1, ((KC) < 9 ? (KC) + 1 : 0), ((KC) < 9), ISSB); } while (0)
#define IA(KC, n) do { if ((n) < 4) B1ISSUE((KC) + 2, 5 + ((n) & 3)); } while (0)
#define IB(KC, n) B1ISSUE((KC) + 3, ((n) + 1) % 5)
#define IA0(n) IA(0, n)
#define IA1(n) IA(1, n)
#define IA2(n) IA(2, n)
#define IA3(n) IA(3, n)
#define IA4(n) IA(4, n)
#define IA5(n) IA(5, n)
#define IA6(n) IA(6, n)
#define IA7(n) IA(7, n)
#define IB0(n) IB(0, n)
#define IB1(n) IB(1, n)
#define IB2(n) IB(2, n)
#define IB3(n) IB(3, n)
#define IB4(n) IB(4, n)
#define IB5(n) IB(5, n)
#define IB6(n) IB(6, n)
#define IC0(n) B2ISSUE(0, ((n) + 1) % 5)
#define IC1(n) B2ISSUE(1, ((n) + 1) % 5)
#define IC2(n) B2ISSUE(2, ((n) + 1) % 5)
#define INONE(n) do {} while (0)
  B1STEP(0, IA0, 9, IB0); B1STEP(1, IA1, 9, IB1); B1STEP(2, IA2, 9, IB2); B1STEP(3, IA3, 9, IB3); B1STEP(4, IA4, 9, IB4);
  B1STEP(5, IA5, 9, IB5); B1STEP(6, IA6, 9, IB6); B1STEP(7, IA7, 9, IC0); B1STEP(8, INONE, 5, IC1); B1STEP(9, IC2, 0, INONE);
  STAMP(7);
  const int part = lane & 1, sub = 2 * w + h, pl0 = i32 >> 1;
  float la[5], lb[5], ko[5], kx[5], k1[5], k2[5], cfr[5], cfi[5];
#pragma unroll
  for (int n = 0; n < 5; ++n) {
    const f32x4 pp = *(LAS f32x4*)(lds + T_PAR + (pl0 + 16 * n) * 32);
    la[n] = pp[0]; lb[n] = part ? pp[1] : -pp[1];
    cfr[n] = pp[2]; cfi[n] = pp[3];
  }
  asm volatile("s_nop 7\n\ts_nop 3" : "+v"(acc[0]), "+v"(acc[1]), "+v"(acc[2]), "+v"(acc[3]), "+v"(acc[4]));
  {
    float s[5] = {acc[0][0], acc[1][0], acc[2][0], acc[3][0], acc[4][0]};
#pragma unroll
    for (int t = 1; t < 16; ++t) {
      float o[5];
      scan_step(o[0], o[1], o[2], o[3], o[4], acc[0][t], acc[1][t], acc[2][t], acc[3][t], acc[4][t], s, la, lb);
#pragma unroll
      for (int n = 0; n < 5; ++n) s[n] = o[n];
    }
#pragma unroll
    for (int n = 0; n < 5; ++n) *(LAS float*)(lds + T_E + (sub * 160 + n * 32 + i32) * 4) = s[n];
  }
  bar();
  STAMP(8);
  const __amdgpu_buffer_rsrc_t rG = mkrs(P.ws + WS_GB1, 128 * 1280);
  unsigned gme = (j * 16 + c) * 1280;
  asm volatile("" : "+s"(gme));
  const f32x4 pq = *(LAS f32x4*)(lds + T_PAR + plc * 32 + 16);
  {
    float hr = 0.f, hi = 0.f;
    f32x2 e[8];
#pragma unroll
    for (int k = 0; k < 8; ++k) e[k] = *(LAS f32x2*)(lds + T_E + (k * 80 + plc) * 8);
    f32x2 lp[8];
#pragma unroll
    for (int k = 0; k < 8; ++k) {
      lp[k] = (f32x2){hr, hi};
      const float t = __builtin_fmaf(pq[2], hr, __builtin_fmaf(-pq[3], hi, e[k][0])); hi = __builtin_fmaf(pq[2], hi, __builtin_fmaf(pq[3], hr, e[k][1])); hr = t;
    }
    const u32x4 gr = {__float_as_uint(hr), tagl, __float_as_uint(hi), tagh};
    if (lane < 20) __builtin_amdgcn_raw_buffer_store_b128(gr, rG, plc * 16, gme, 16);
#pragma unroll
    for (int k = 0; k < 8; ++k) *(LAS f32x2*)(lds + T_L + (k * 80 + plc) * 8) = lp[k];
  }
  STAMP(9);
#pragma unroll
  for (int n = 0; n < 5; ++n) {
    const f32x2 pw = *(LAS f32x2*)(lds + T_POW + (sub * 80 + pl0 + 16 * n) * 8);
    ko[n] = part ? -2.f * cfr[n] : 2.f * cfr[n]; kx[n] = -2.f * cfi[n];
    const float wr_ = 2.f * (cfr[n] * pw[0] - cfi[n] * pw[1]), wi_ = -2.f * (cfr[n] * pw[1] + cfi[n] * pw[0]);
    k1[n] = part ? wi_ : wr_; k2[n] = part ? -wr_ : wi_;
    PIN2(k1[n], k2[n]);
  }
#pragma unroll
  for (int x = 0; x < 20; ++x) {
    const int n = x >> 2, q = (x & 3) * 4;
    float u0 = acc[n][q], u1 = acc[n][q + 1], u2 = acc[n][q + 2], u3 = acc[n][q + 3], t0, t1, t2, t3;
    asm volatile("s_nop 1\n\tv_mul_f32_dpp %4, %0, %8 quad_perm:[1,0,3,2] row_mask:0xf bank_mask:0xf\n\tv_mul_f32_dpp %5, %1, %8 quad_perm:[1,0,3,2] row_mask:0xf bank_mask:0xf\n\t"
                 "v_mul_f32_dpp %6, %2, %8 quad_perm:[1,0,3,2] row_mask:0xf bank_mask:0xf\n\tv_mul_f32_dpp %7, %3, %8 quad_perm:[1,0,3,2] row_mask:0xf bank_mask:0xf\n\t"
                 "v_fma_f32 %0, %0, %9, %4\n\tv_fma_f32 %1, %1, %9, %5\n\tv_fma_f32 %2, %2, %9, %6\n\tv_fma_f32 %3, %3, %9, %7"
                 : "+v"(u0), "+v"(u1), "+v"(u2), "+v"(u3), "=&v"(t0), "=&v"(t1), "=&v"(t2), "=&v"(t3) : "v"(kx[n]), "v"(ko[n]));
    acc[n][q] = u0; acc[n][q + 1] = u1; acc[n][q + 2] = u2; acc[n][q + 3] = u3;
  }
  STAMP(10);
  {
    float hr = 0.f, hi = 0.f;
    u32x4 gv[15];
    for (unsigned sp = 0; sp < SPIN_CAP; ++sp) {
      bool ok = true;
#pragma unroll
      for (int k = 0; k < 15; ++k)
        gv[k] = __builtin_amdgcn_raw_buffer_load_b128(rG, plc * 16, c - 15 + k >= 0 ? gme - (15 - k) * 1280 : 0x7fffff00, 16);
#pragma unroll
      for (int k = 0; k < 15; ++k) ok &= (c - 15 + k < 0) | ((gv[k][1] == tagl) & (gv[k][3] == tagh));
      if (__all(ok)) break;
      asm volatile("" ::: "memory");
    }
    STAMP(11);
#pragma unroll
    for (int k = 0; k < 15; ++k) {
      const float t = __builtin_fmaf(pq[0], hr, __builtin_fmaf(-pq[1], hi, __uint_as_float(gv[k][0])));
      hi = __builtin_fmaf(pq[0], hi, __builtin_fmaf(pq[1], hr, __uint_as_float(gv[k][2]))); hr = t;
    }
    *(LAS f32x2*)(lds + T_H + plc * 8) = (f32x2){hr, hi};
  }
  bar();
  STAMP(12);
  float s[5];
#pragma unroll
  for (int n = 0; n < 5; ++n) {
    const f32x2 L = *(LAS f32x2*)(lds + T_L + (sub * 80 + pl0 + 16 * n) * 8);
    const f32x2 H = *(LAS f32x2*)(lds + T_H + (pl0 + 16 * n) * 8);
    const float lg = part ? -2.f * (cfr[n] * L[1] + cfi[n] * L[0]) : 2.f * (cfr[n] * L[0] - cfi[n] * L[1]);
    s[n] = lg + k1[n] * H[0] + k2[n] * H[1];
    lb[n] = -lb[n];
  }
  bar();
#pragma unroll
  for (int t = 0; t < 16; ++t) {
    float o[5];
    scan_step(o[0], o[1], o[2], o[3], o[4], acc[0][t], acc[1][t], acc[2][t], acc[3][t], acc[4][t], s, la, lb);
#pragma unroll
    for (int n = 0; n < 5; ++n) { s[n] = o[n]; acc[n][t] = o[n]; }
    PIN1(s[0]);
    if (t == 0) B2ISSUE(3, 0); if (t == 1) B2ISSUE(3, 1); if (t == 2) B2ISSUE(3, 2); if (t == 3) B2ISSUE(3, 3); if (t == 4) B2ISSUE(3, 4);
    if (t == 5) B2ISSUE(4, 0); if (t == 6) B2ISSUE(4, 1); if (t == 7) B2ISSUE(4, 2); if (t == 8) B2ISSUE(4, 3); if (t == 9) B2ISSUE(4, 4);
    SCHED();
  }
  const unsigned trw = TB + w * 12800 + i32 * 80 + h * 32;
#pragma unroll
  for (int n = 0; n < 5; ++n) {
#pragma unroll
    for (int hh = 0; hh < 2; ++hh) {
      const u32x4 v = {pk2(acc[n][8 * hh], acc[n][8 * hh + 1]), pk2(acc[n][8 * hh + 2], acc[n][8 * hh + 3]),
                       pk2(acc[n][8 * hh + 4], acc[n][8 * hh + 5]), pk2(acc[n][8 * hh + 6], acc[n][8 * hh + 7])};
      *(LAS u32x4*)(lds + trw + n * 2560 + hh * 16) = v;
    }
  }
  const __amdgpu_buffer_rsrc_t rH = mkrs(P.ws + WS_HB + c * 327680, 327680);
  const unsigned vhl = w * 1024 + lane * 16;
  {
    const unsigned trr = TB + w * 12800 + (16 * h + 2 * (i >> 2)) * 80 + (g & 1) * 32 + (i & 3) * 8;
#pragma unroll
    for (int n = 0; n < 5; ++n)
#pragma unroll
      for (int pt = 0; pt < 2; ++pt) {
        const bf16x8 v = cat(ldstr(lds, trr + n * 2560 + pt * 80), ldstr(lds, trr + n * 2560 + pt * 80 + 640));
        __builtin_amdgcn_raw_buffer_store_b128(__builtin_bit_cast(u32x4, v), rH, vhl, (pt * 40 + j * 5 + n) * 4096, 16);
      }
  }
  STAMP(13);
  vmwait<0>(); bar();
  if (w == 0 && lane == 0) __hip_atomic_store((GAS u64*)(P.ws + WS_FB2 + (c * 8 + j) * 128), tag, __ATOMIC_RELAXED, __HIP_MEMORY_SCOPE_AGENT);
  STAMP(14);
  f32x16 y[2];
  f32x4 z[2];
  {
    const unsigned d2 = *(const unsigned*)(P.dsk + j * 80 + 2 * i32);
    const float d0 = bf2f(d2 & 0xffffu), d1 = bf2f(d2 >> 16), d4 = bf2f(P.dsk[j * 80 + 64 + i]);
    const u16* xr = P.Bx + (c * 128 + 32 * w) * 640 + j * 80;
    unsigned xa[16], xb[8];
#pragma unroll
    for (int r = 0; r < 16; ++r) xa[r] = *(const unsigned*)(xr + ((r & 3) + 8 * (r >> 2) + 4 * h) * 640 + 2 * i32);
#pragma unroll
    for (int r = 0; r < 8; ++r) xb[r] = xr[(16 * (r >> 2) + 4 * g + (r & 3)) * 640 + 64 + i];
    SCHED();
    B2ISSUE(5, 0); B2ISSUE(5, 1); B2ISSUE(5, 2); B2ISSUE(5, 3); B2ISSUE(5, 4);
    B2ISSUE(6, 0); B2ISSUE(6, 1); B2ISSUE(6, 2); B2ISSUE(6, 3); B2ISSUE(6, 4);
#pragma unroll
    for (int r = 0; r < 16; ++r) { y[0][r] = rbf(bf2f(xa[r] & 0xffffu) * d0); y[1][r] = rbf(bf2f(xa[r] >> 16) * d1); }
#pragma unroll
    for (int r = 0; r < 8; ++r) z[r >> 2][r & 3] = rbf(bf2f(xb[r]) * d4);
    asm volatile("" : "+v"(y[0]), "+v"(y[1]), "+v"(z[0]), "+v"(z[1]));
  }
  STAMP(15);
  poll_flags(P.ws + WS_FB2 + c * 1024, 128, 8, lane, tag);
  STAMP(16);
  if (c == 15 && j == 0 && w == 0 && lane == 0) __hip_atomic_store((GAS u64*)(P.ws + WS_EPOCH_B), tag, __ATOMIC_RELAXED, __HIP_MEMORY_SCOPE_AGENT);
#define HAUX 16
#define XPD 6
  constexpr int PD = XPD;
  u32x4 hf_[PD][2];
  unsigned kc3 = rot * 128;
#define B2HLD(KS) do { const unsigned so_ = ((((KS) >> 1) & 1) * 40 + ((KS) & 1) * 2) * 4096 + kc3 * 128; \
                       hf_[(KS) % PD][0] = __builtin_amdgcn_raw_buffer_load_b128(rH, vhl, so_, HAUX); \
                       hf_[(KS) % PD][1] = __builtin_amdgcn_raw_buffer_load_b128(rH, vhl, so_ + 4096, HAUX); if (((KS) & 3) == 3) kadv(kc3); } while (0)
#pragma unroll
  for (int ks = 0; ks < PD - 1; ++ks) B2HLD(ks);
  LBase<6, 3> aC;
#pragma unroll
  for (int s = 0; s < 4; ++s) aC.v[0][s] = i32 * 128 + (((2 * s + h) ^ ma) << 4);
#pragma unroll
  for (int s = 0; s < 2; ++s) aC.v[0][4 + s] = (64 + i) * 128 + (((4 * s + 2 * (g & 1) + (g >> 1)) ^ (i >> 1)) << 4);
  aC.fin();
  bf16x8 fc[2][5];
#define B2RD(KS, N) fc[(KS) & 1][N] = ldsr128(lds, aC.at<cbase((KS) >> 2, ((KS) >> 1) & 1) + ((N) < 4 ? ((N) & 1) * 4096 : 0)>((N) < 4 ? 2 * ((KS) & 1) + ((N) >> 1) : 4 + ((KS) & 1)))
  B2RD(0, 0); B2RD(0, 1); B2RD(0, 2); B2RD(0, 3); B2RD(0, 4);
  SCHED();
#define B2LATE(N) do { if constexpr ((N) >= 0) B2ISSUE(7 + ((N) < 0 ? 0 : (N)) / 5, ((N) < 0 ? 0 : (N)) % 5); } while (0)
#define X2 0
#define B2HLD1(KS, Q) do { if constexpr ((KS) < 40 && !(X2 & 1)) { const unsigned so_ = ((((KS) >> 1) & 1) * 40 + ((KS) & 1) * 2 + (Q)) * 4096 + kc3 * 128; \
                       hf_[(KS) % PD][Q] = __builtin_amdgcn_raw_buffer_load_b128(rH, vhl, so_, HAUX); if (((KS) & 3) == 3 && (Q) == 1) kadv(kc3); } } while (0)
#define B2RDX(KS, N) do { if constexpr ((KS) < 40 && !(X2 & 2)) B2RD(KS, N); } while (0)
#define B2STEP(KS) do { \
    if ((KS) == 27) vmwait<10>(); \
    if ((KS) == 11 || (KS) == 27) bar();   \
    u32x4 f0 = hf_[(KS) % PD][0], f1 = hf_[(KS) % PD][1]; \
    mfma32(y[0], __builtin_bit_cast(bf16x8, f0), fc[(KS) & 1][0]); \
    B2RDX((KS) + 1, 0); B2RDX((KS) + 1, 1); B2RDX((KS) + 1, 2); \
    SCHED(); \
    mfma32(y[1], __builtin_bit_cast(bf16x8, f0), fc[(KS) & 1][1]); \
    B2HLD1((KS) + PD - 1, 0); \
    SCHED(); \
    mfma32(y[0], __builtin_bit_cast(bf16x8, f1), fc[(KS) & 1][2]); \
    B2HLD1((KS) + PD - 1, 1); \
    SCHED(); \
    mfma32(y[1], __builtin_bit_cast(bf16x8, f1), fc[(KS) & 1][3]); \
    if constexpr (!(X2 & 4)) swap16x4(f0, f1); \
    B2RDX((KS) + 1, 3); B2RDX((KS) + 1, 4); \
    SCHED(); \
    mfma16x2(z[0], z[1], f0, f1, fc[(KS) & 1][4]); \
    B2LATE(late_dma(KS, 0)); B2LATE(late_dma(KS, 1)); \
    SCHED(); } while (0)
#define B2STEP4(K) do { B2STEP(K); B2STEP(K + 1); B2STEP(K + 2); B2STEP(K + 3); } while (0)
  B2STEP4(0); B2STEP4(4); B2STEP4(8); B2STEP4(12); B2STEP4(16); B2STEP4(20); B2STEP4(24); B2STEP4(28); B2STEP4(32); B2STEP4(36);
  STAMP(17);
  asm volatile("s_nop 7\n\ts_nop 3" : "+v"(y[0]), "+v"(y[1]), "+v"(z[0]), "+v"(z[1]));
  {
    const __amdgpu_buffer_rsrc_t rO = mkrs(P.out, 2048 * 5120);
    const unsigned so = (c * 128 + 32 * w) * 5120 + (640 + j * 80) * 4;
#pragma unroll
    for (int r = 0; r < 16; ++r) {
      const u32x2 v = {__float_as_uint(y[0][r]), __float_as_uint(y[1][r])};
      __builtin_amdgcn_raw_buffer_store_b64(v, rO, h * 20480 + i32 * 8, so + ((r & 3) + 8 * (r >> 2)) * 5120, 16);
    }
#pragma unroll
    for (int r = 0; r < 8; ++r)
      __builtin_amdgcn_raw_buffer_store_b32(__float_as_uint(z[r >> 2][r & 3]), rO, g * 20480 + 256 + i * 4, so + (16 * (r >> 2) + (r & 3)) * 5120, 16);
  }
}

constexpr int RINGA = 36928, A_Q0 = RINGA, A_R1 = 118848, A_WPHI = 151616, A_KVT = 154816, A_ZERO = 161536, A_ONES = 163072;
constexpr int qbase(int qc) { return qc < 4 ? A_Q0 + qc * 20480 : 0; }
DI void mfma32a(f32x16& c, bf16x8 a, bf16x8 b) { asm volatile("v_mfma_f32_32x32x16_bf16 %0, %1, %2, %0" : "+v"(c) : "v"(a), "a"(b)); }
DI void mfma32az(f32x16& c, bf16x8 a, bf16x8 b) { asm volatile("v_mfma_f32_32x32x16_bf16 %0, %1, %2, 0" : "=&v"(c) : "v"(a), "a"(b)); }
DI void mfma32fa(f32x16& c, bf16x8 a, u32x4 b) { asm volatile("v_mfma_f32_32x32x16_bf16 %0, %1, %2, %0" : "+v"(c) : "a"(a), "v"(b)); }
DI void mfma32faz(f32x16& c, bf16x8 a, u32x4 b) { asm volatile("v_mfma_f32_32x32x16_bf16 %0, %1, %2, 0" : "=&v"(c) : "a"(a), "v"(b)); }
DI void mfma16z(f32x4& c, bf16x8 a, bf16x8 b) { asm volatile("v_mfma_f32_16x16x32_bf16 %0, %1, %2, 0" : "=&v"(c) : "v"(a), "v"(b)); }
#define NOP12(...) asm volatile("s_nop 7\n\ts_nop 3" : __VA_ARGS__)
DI u32x4 pk8(const f32x16& a, int o) { return (u32x4){pk2(a[o], a[o + 1]), pk2(a[o + 2], a[o + 3]), pk2(a[o + 4], a[o + 5]), pk2(a[o + 6], a[o + 7])}; }
DI u32x4 pk44(const f32x16& a, int oa, const f32x16& b, int ob) { return (u32x4){pk2(a[oa], a[oa + 1]), pk2(a[oa + 2], a[oa + 3]), pk2(b[ob], b[ob + 1]), pk2(b[ob + 2], b[ob + 3])}; }
constexpr int fidx(int T, int u) { return T == 0 ? (u == 4 ? 3 : u) : T == 1 ? 3 + u : 8 + u; }
constexpr bool isfirst(int kh, int m0) { return kh == 0 ? m0 < 5 : (m0 < 3 || m0 >= 8); }

DI void dual_softmax(const float (&p)[20], u32x4 (&o)[5]) {
  constexpr float L2E = 1.4426950408889634f;
  float mx = p[0], mn = p[0];
#pragma unroll
  for (int k = 1; k < 20; ++k) { mx = fmaxf(mx, p[k]); mn = fminf(mn, p[k]); }
  const float cx = -mx * L2E, cn = mn * L2E;
  float e[40], sp = 0.f, sn = 0.f;
#pragma unroll
  for (int k = 0; k < 20; ++k) {
    e[k] = __builtin_amdgcn_exp2f(__builtin_fmaf(p[k], L2E, cx));
    e[20 + k] = __builtin_amdgcn_exp2f(__builtin_fmaf(p[k], -L2E, cn));
    sp += e[k]; sn += e[20 + k];
  }
  const float rp = __builtin_amdgcn_rcpf(sp), rn = __builtin_amdgcn_rcpf(sn);
#pragma unroll
  for (int k = 0; k < 20; ++k) { e[k] *= rp; e[20 + k] *= rn; }
#pragma unroll
  for (int u = 0; u < 5; ++u) o[u] = (u32x4){pk2(e[8 * u], e[8 * u + 1]), pk2(e[8 * u + 2], e[8 * u + 3]), pk2(e[8 * u + 4], e[8 * u + 5]), pk2(e[8 * u + 6], e[8 * u + 7])};
}
DI void feature_mfma(const f32x16& a0, const f32x16& a1, const f32x16& a2, const bf16x8 (&wf)[13], f32x16 (&pp)[3]) {
  u32x4 kb[5] = {pk8(a0, 0), pk8(a0, 8), pk8(a1, 0), pk8(a1, 8), pk8(a2, 0)};
  asm volatile("s_nop 1" : "+v"(kb[0]), "+v"(kb[1]), "+v"(kb[2]), "+v"(kb[3]), "+v"(kb[4]));
  mfma32faz(pp[0], wf[fidx(0, 0)], kb[0]); mfma32faz(pp[1], wf[fidx(1, 1)], kb[1]); mfma32faz(pp[2], wf[fidx(2, 0)], kb[0]);
  mfma32fa(pp[0], wf[fidx(0, 1)], kb[1]); mfma32fa(pp[1], wf[fidx(1, 2)], kb[2]); mfma32fa(pp[2], wf[fidx(2, 1)], kb[1]);
  mfma32fa(pp[0], wf[fidx(0, 2)], kb[2]); mfma32fa(pp[1], wf[fidx(1, 3)], kb[3]); mfma32fa(pp[2], wf[fidx(2, 2)], kb[2]);
  mfma32fa(pp[2], wf[fidx(2, 3)], kb[3]);
  mfma32fa(pp[0], wf[fidx(0, 4)], kb[4]); mfma32fa(pp[2], wf[fidx(2, 4)], kb[4]); mfma32fa(pp[1], wf[fidx(1, 4)], kb[4]);
  NOP12("+v"(pp[0]), "+v"(pp[1]), "+v"(pp[2]));
}
template <int HD> DI void head_vals(const f32x16 (&pp)[3], float (&p)[20]) {
#pragma unroll
  for (int k = 0; k < 16; ++k) p[k] = pp[HD][k];
#pragma unroll
  for (int k = 0; k < 4; ++k) p[16 + k] = pp[2][4 * HD + k];
}

DI void chain_a(const Params& P, LAS char* lds, int c, int G, int rot, int sh, int w, int lane PROF_ARG) {
  const int g = lane >> 4, i = lane & 15, h = lane >> 5, i32 = lane & 31, e3 = lane >> 3, pos = lane & 7, q = i >> 2, p = i & 3;
  const u32x4 sX = mksrd(P.Ax, 2048 * 1280), sW = mksrd(P.wqkv, 640 * 3840);
  const unsigned vx0 = e3 * 1280 + ((pos ^ (e3 >> 1)) << 4), vx1 = e3 * 1280 + ((pos ^ (4 + (e3 >> 1))) << 4);
  unsigned pk_[5], pu_[5], vW[5];
#pragma unroll
  for (int u = 0; u < 5; ++u) {
    const unsigned U = 64 * u + lane, pr = (U * 6554u) >> 16;
    pu_[u] = U - 10 * pr; pk_[u] = (pr & ~7u) | ((pr & 1u) << 2) | ((pr & 7u) >> 1);
    vW[u] = pk_[u] * 3840 + pu_[u] * 16;
  }
  const unsigned sx0 = (c * 128 + 32 * w) * 1280;
  const unsigned swq = (w & 1) * 32 * 3840 + G * 160, sw0 = swq + (1 + (w >> 1)) * 1280;
  const unsigned lbx = w * 4096, lbq = w * 5120, lbw = 16384 + lbq + (w >> 1) * 32;
  unsigned kc1 = rot * 128;
#define A1ISSUE(KC, U) do { constexpr int sl_ = ((KC) % 3) * RINGA; \
    if constexpr ((U) < 4) dma16<sl_ + (U) * 1024, (U) * 10240>(sX, ((U) & 1) ? vx1 : vx0, sx0 + kc1, lbx); \
    else dma16<sl_ + ((U) - 4) * 1024, 0>(sW, vW[(U) >= 4 ? (U) - 4 : 0], sw0 + kc1 * 1920, lbw); \
    if ((U) == 8) kadv(kc1); } while (0)
#define A1ALL(KC) do { A1ISSUE(KC, 0); A1ISSUE(KC, 4); A1ISSUE(KC, 5); A1ISSUE(KC, 1); A1ISSUE(KC, 6); A1ISSUE(KC, 2); A1ISSUE(KC, 7); A1ISSUE(KC, 3); A1ISSUE(KC, 8); } while (0)
  A1ALL(0);
  dma16<A_WPHI, 0>(mksrd(P.wphi, 32 * 800), lane * 16, G * 3200 + w * 1024, w * 1024);
  u64 tag = *(const CAS u64*)(P.ws + WS_EPOCH_A);
  if (w == 0) { *(LAS u32x4*)(lds + A_ZERO + lane * 16) = (u32x4){0u, 0u, 0u, 0u}; if (lane < 32) *(LAS u32x4*)(lds + A_ZERO + 1024 + lane * 16) = (u32x4){0u, 0u, 0u, 0u}; }
  if (w == 1 && lane == 0) *(LAS u32x4*)(lds + A_ONES) = (u32x4){0x3f80u, 0u, 0u, 0u};
  unsigned kq = rot * 128;
  if (w >= 2) kadv(kq);
#define AQISSUE(QC, U) do { dma16<qbase(QC) + (U) * 1024, 0>(sW, vW[U], swq + kq * 1920, lbq); if ((U) == 4) { kadv(kq); kadv(kq); } } while (0)
  LBase<4, 2> aX;
#pragma unroll
  for (int s = 0; s < 4; ++s) aX.v[0][s] = (w * 32 + i32) * 128 + (((2 * s + h) ^ ((i32 >> 1) & 7)) << 4);
  aX.fin();
  LBase<6, 2> aT;
  {
    const unsigned x = 2 * (g & 1) + (p >> 1), hh = p & 1, rowb = (8 * (g >> 1) + 2 * q) * 160;
    aT.v[0][0] = rowb + 8 * (x + 12 * hh);
    aT.v[0][1] = rowb + 8 * (4 + x + 12 * hh);
    aT.v[0][2] = rowb + (x < 2 ? 8 * (8 + x + 2 * hh) : 10272 + 8 * (x - 2 + 10 * hh));
    aT.v[0][3] = rowb + 10272 + 8 * (2 + x + 12 * hh);
    aT.v[0][4] = rowb + 10272 + 8 * (hh ? (x < 2 ? 12 + x : 16 + x) : 6 + x);
    aT.v[0][5] = rowb + 8 * (x < 2 ? 8 + x + 2 * hh : 2 + x + 10 * hh);
  }
  aT.fin();
  SCHED();
  A1ALL(1);
  STAMP(1);
  vmwait<9>(); bar();
  STAMP(2);
  asm volatile("" : "+s"(tag));
  tag += 0x9E3779B97F4A7C15ull;
  const unsigned tagl = (unsigned)tag, tagh = (unsigned)(tag >> 32);
  bf16x8 xf[40], fw[4][5];
  f32x16 acc[5];
#define A1RDW(KC, S, T) fw[S][T] = cat(ldstr(lds, aT.at<((KC) % 3) * RINGA + 16384 + (S) * 2560>(T)), ldstr(lds, aT.at<((KC) % 3) * RINGA + 16384 + (S) * 2560 + 160>(T)))
#define A1RDX(KC, S) xf[4 * (KC) + (S)] = ldsr128(lds, aX.at<((KC) % 3) * RINGA>(S))
#define A1RDG(KC, HF, N) do { constexpr int s0_ = 2 * (HF), s1_ = 2 * (HF) + 1; \
    if ((N) == 0) { A1RDX(KC, s0_); A1RDW(KC, s0_, 0); } if ((N) == 1) { A1RDW(KC, s0_, 1); A1RDW(KC, s0_, 2); } if ((N) == 2) { A1RDW(KC, s0_, 3); } \
    if ((N) == 3) { A1RDW(KC, s0_, 4); A1RDX(KC, s1_); } if ((N) == 4) { A1RDW(KC, s1_, 0); } if ((N) == 5) { A1RDW(KC, s1_, 1); A1RDW(KC, s1_, 2); } \
    if ((N) == 6) { A1RDW(KC, s1_, 3); } if ((N) == 7) { A1RDW(KC, s1_, 4); } } while (0)
  A1RDG(0, 0, 0); A1RDG(0, 0, 1); A1RDG(0, 0, 2); A1RDG(0, 0, 3); A1RDG(0, 0, 4); A1RDG(0, 0, 5); A1RDG(0, 0, 6); A1RDG(0, 0, 7);
  bf16x8 wf[13];
  {
    const bool same = (p & 1) == h;
    const unsigned xo = 2 * (g & 1) + (p >> 1);
    const unsigned b01 = A_WPHI + q * 40 + 8 * xo, b2 = A_WPHI + q * 40 + 32;
#define WFADDR(T, M0) ((same && ((T) == 2 ? (xo < 2 && ((xo == 0) == (h ? isfirst(1, M0) : isfirst(0, M0)))) : (h ? isfirst(1, M0) == ((T) == 0) : isfirst(0, M0) == ((T) == 0)))) \
                           ? ((T) == 2 ? b2 : b01) + (h ? ((M0) < 8 ? 1920 : 320) : 0) : (unsigned)A_ZERO)
#define WFBUILD(T, U) wf[fidx(T, U)] = cat(ldstr(lds, WFADDR(T, 2 * (U)) + 320 * (U)), ldstr(lds, WFADDR(T, 2 * (U) + 1) + 320 * (U) + 160))
    WFBUILD(0, 0); WFBUILD(0, 1); WFBUILD(0, 2); WFBUILD(0, 4); WFBUILD(1, 1); WFBUILD(1, 2); WFBUILD(1, 3); WFBUILD(1, 4);
    WFBUILD(2, 0); WFBUILD(2, 1); WFBUILD(2, 2); WFBUILD(2, 3); WFBUILD(2, 4);
#pragma unroll
    for (int k = 0; k < 13; ++k) asm volatile("" : "+a"(wf[k]));
  }
  SCHED();
#define A1GAP(KC, HF, X, RKC, RD, ISS) do { constexpr int s_ = 2 * (HF) + (X) / 5; \
    if ((KC) == 0 && (HF) == 0 && (X) < 5) mfma32az(acc[(X) % 5], fw[s_][(X) % 5], xf[4 * (KC) + s_]); \
    else mfma32a(acc[(X) % 5], fw[s_][(X) % 5], xf[4 * (KC) + s_]); \
    if constexpr (RD && (X) < 8) A1RDG(RKC, 1 - (HF), X); \
    if ((KC) == 0 && (HF) == 0) { if ((X) >= 1) A1ISSUE(2, ((X) >= 1 ? (X) - 1 : 0)); } \
    else { if ((X) == 2) { ISS(0); } if ((X) == 4) { ISS(1); } if ((X) == 6) { ISS(2); } if ((X) == 8) { ISS(3); } if ((X) == 9) { ISS(4); } } \
    SCHED(); } while (0)
#define A1HALF(KC, HF, RKC, RD, ISS) do { A1GAP(KC, HF, 0, RKC, RD, ISS); A1GAP(KC, HF, 1, RKC, RD, ISS); A1GAP(KC, HF, 2, RKC, RD, ISS); A1GAP(KC, HF, 3, RKC, RD, ISS); \
    A1GAP(KC, HF, 4, RKC, RD, ISS); A1GAP(KC, HF, 5, RKC, RD, ISS); A1GAP(KC, HF, 6, RKC, RD, ISS); A1GAP(KC, HF, 7, RKC, RD, ISS); A1GAP(KC, HF, 8, RKC, RD, ISS); A1GAP(KC, HF, 9, RKC, RD, ISS); } while (0)
#define A1STEP(KC, ISSA, VM, ISSB) do { A1HALF(KC, 0, KC, true, ISSA); if ((KC) < 9) vmwait<VM>(); bar(); A1HALF(KC, 1, ((KC) < 9 ? (KC) + 1 : 0), ((KC) < 9), ISSB); } while (0)
#define JA(KC, n) do { if constexpr ((n) < 4) A1ISSUE((KC) + 2, 5 + ((n) & 3)); } while (0)
#define JB(KC, n) A1ISSUE((KC) + 3, n)
#define JA1(n) JA(1, n)
#define JA2(n) JA(2, n)
#define JA3(n) JA(3, n)
#define JA4(n) JA(4, n)
#define JA5(n) JA(5, n)
#define JA6(n) JA(6, n)
#define JA7(n) JA(7, n)
#define JB0(n) JB(0, n)
#define JB1(n) JB(1, n)
#define JB2(n) JB(2, n)
#define JB3(n) JB(3, n)
#define JB4(n) JB(4, n)
#define JB5(n) JB(5, n)
#define JB6(n) JB(6, n)
#define JQ0(n) AQISSUE(0, n)
#define JQ1(n) AQISSUE(1, n)
#define JQ2(n) AQISSUE(2, n)
#define JQ3(n) AQISSUE(3, n)
  A1STEP(0, INONE, 9, JB0); A1STEP(1, JA1, 9, JB1); A1STEP(2, JA2, 9, JB2); A1STEP(3, JA3, 9, JB3); A1STEP(4, JA4, 9, JB4);
  A1STEP(5, JA5, 9, JB5); A1STEP(6, JA6, 9, JB6); A1STEP(7, JA7, 9, JQ0); A1STEP(8, INONE, 5, JQ1); A1STEP(9, JQ2, 0, JQ3);
  STAMP(3);
  NOP12("+v"(acc[0]), "+v"(acc[1]), "+v"(acc[2]), "+v"(acc[3]), "+v"(acc[4]));
  const unsigned rg = (w < 2 ? 0 : A_R1) + (32 * (w & 1) + i32) * 352, rv = (w < 2 ? 0 : A_R1) + 22528 + (32 * (w & 1) + i32) * 160;
  {
    f32x16 pp[3];
    feature_mfma(acc[0], acc[1], acc[2], wf, pp);
    *(LAS u32x4*)(lds + rv + 80 * h) = pk8(acc[2], 8);
    *(LAS u32x4*)(lds + rv + 16 + 96 * h) = pk8(acc[3], 0);
    *(LAS u32x4*)(lds + rv + 32 + 96 * h) = pk8(acc[3], 8);
    *(LAS u32x4*)(lds + rv + 48 + 48 * h) = pk8(acc[4], 0);
    *(LAS u32x4*)(lds + rv + 64 + 80 * h) = pk8(acc[4], 8);
    float pv[20];
    u32x4 ph[5];
    head_vals<0>(pp, pv); dual_softmax(pv, ph);
#pragma unroll
    for (int u = 0; u < 5; ++u) *(LAS u32x4*)(lds + rg + 160 * h + 16 * u) = ph[u];
    head_vals<1>(pp, pv); dual_softmax(pv, ph);
#pragma unroll
    for (int u = 0; u < 5; ++u) *(LAS u32x4*)(lds + rg + 160 * h + 80 + 16 * u) = ph[u];
  }
  bar();
  STAMP(4);
  const __amdgpu_buffer_rsrc_t rP = mkrs(P.ws + WS_KVP + (G * 4 + w) * 65536, 65536);
  {
    const unsigned tr = 16 * (g >> 1) + 4 * (g & 1) + q;
    LBase<1, 3> aP, aV;
    aP.v[0][0] = tr * 352 + 80 * w + 8 * p; aV.v[0][0] = 22528 + tr * 160 + 40 * w + 8 * p;
    aP.fin(); aV.fin();
    const unsigned v1b = p == 1 ? (unsigned)A_ONES : aV.v[0][0] + 32, v1m = p == 1 ? 0u : 1u;
    f32x4 kv[3][2];
#define KVSTEP(KS) do { constexpr int ro_ = ((KS) >> 1) * A_R1, r0_ = 32 * ((KS) & 1); \
      bf16x8 a_[3], b_[2]; \
      a_[0] = cat(ldstr(lds, aP.at<ro_ + r0_ * 352>(0)), ldstr(lds, aP.at<ro_ + (r0_ + 8) * 352>(0))); \
      b_[0] = cat(ldstr(lds, aV.at<ro_ + r0_ * 160>(0)), ldstr(lds, aV.at<ro_ + (r0_ + 8) * 160>(0))); \
      b_[1] = cat(ldstr(lds, v1b + v1m * (ro_ + r0_ * 160)), ldstr(lds, v1b + v1m * (ro_ + (r0_ + 8) * 160))); \
      a_[1] = cat(ldstr(lds, aP.at<ro_ + r0_ * 352 + 32>(0)), ldstr(lds, aP.at<ro_ + (r0_ + 8) * 352 + 32>(0))); \
      a_[2] = cat(ldstr(lds, aP.at<ro_ + r0_ * 352 + 64>(0)), ldstr(lds, aP.at<ro_ + (r0_ + 8) * 352 + 64>(0))); \
      _Pragma("unroll") for (int m = 0; m < 3; ++m) _Pragma("unroll") for (int n = 0; n < 2; ++n) { if ((KS) == 0) mfma16z(kv[m][n], a_[m], b_[n]); else mfma16(kv[m][n], a_[m], b_[n]); } } while (0)
    KVSTEP(0); KVSTEP(1); KVSTEP(2); KVSTEP(3);
    asm volatile("s_nop 7" : "+v"(kv[0][0]), "+v"(kv[0][1]), "+v"(kv[1][0]), "+v"(kv[1][1]), "+v"(kv[2][0]), "+v"(kv[2][1]));
#pragma unroll
    for (int m = 0; m < 3; ++m)
#pragma unroll
      for (int n = 0; n < 2; ++n) {
        const bool ok = (m < 2 || g < 2) && (n == 0 || i <= 4);
        __builtin_amdgcn_raw_buffer_store_b128(__builtin_bit_cast(u32x4, kv[m][n]), rP, ok ? ((4 * m + g) * 21 + 16 * n + i) * 16 : 0x7fffff00, c * 4096, 16);
      }
  }
  vmwait<6>(); bar();
  STAMP(5);
  AQISSUE(4, 0); AQISSUE(4, 1); AQISSUE(4, 2); AQISSUE(4, 3); AQISSUE(4, 4);
  f32x16 aq[3];
  bf16x8 fq[3][3];
  u32x4 hp[4];
#define AQOFF(K) (qbase((K) >> 3) + (((K) >> 2) & 1) * 10240 + ((K) & 3) * 2560)
#define AQRD(K, T) fq[(K) % 3][T] = cat(ldstr(lds, aT.at<AQOFF(K)>((T) == 2 ? 5 : (T))), ldstr(lds, aT.at<AQOFF(K) + 160>((T) == 2 ? 5 : (T))))
  AQRD(0, 0); AQRD(0, 1); AQRD(0, 2); AQRD(1, 0); AQRD(1, 1); AQRD(1, 2);
  SCHED();
#define AQGAP(K, T) do { if ((K) == 0) mfma32az(aq[T], fq[(K) % 3][T], xf[K]); else mfma32a(aq[T], fq[(K) % 3][T], xf[K]); \
    if constexpr ((K) + 2 < 40) AQRD((K) + 2, T); SCHED(); } while (0)
#define AQSTEP(K) do { AQGAP(K, 0); AQGAP(K, 1); AQGAP(K, 2); } while (0)
#define AQSTEP5(K) do { AQSTEP(K); AQSTEP(K + 1); AQSTEP(K + 2); AQSTEP(K + 3); AQSTEP(K + 4); } while (0)
  AQSTEP5(0); AQSTEP5(5); AQSTEP5(10);
  vmwait<5>();
  if (lane == 0) __hip_atomic_store((GAS u64*)(P.ws + WS_FA1 + ((G * 4 + w) * 16 + c) * 128), tag, __ATOMIC_RELAXED, __HIP_MEMORY_SCOPE_AGENT);
  SCHED();
  AQSTEP5(15); AQSTEP5(20); AQSTEP5(25);
  STAMP(6);
  poll_flags(P.ws + WS_FA1 + (G * 4 + w) * 2048, 128, 16, lane, tag);
  bar();
  STAMP(7);
  {
    const unsigned vo = g * 16384 + i * 16;
    const u32x4 sP = mksrd(P.ws + WS_KVP + (G * 4 + w) * 65536, 65536);
    const unsigned s0 = c * 256, s1 = s0 + 4096, s2 = s0 + 8192, s3 = s0 + 12288;
    asm volatile("s_nop 4\n\tbuffer_load_dwordx4 %0, %4, %5, %6 offen sc1\n\tbuffer_load_dwordx4 %1, %4, %5, %7 offen sc1\n\t"
                 "buffer_load_dwordx4 %2, %4, %5, %8 offen sc1\n\tbuffer_load_dwordx4 %3, %4, %5, %9 offen sc1"
                 : "=&v"(hp[0]), "=&v"(hp[1]), "=&v"(hp[2]), "=&v"(hp[3]) : "v"(vo), "s"(sP), "s"(s0), "s"(s1), "s"(s2), "s"(s3) : "memory");
  }
  SCHED();
  AQSTEP5(30); AQSTEP5(35);
  bar();
  STAMP(8);
  NOP12("+v"(aq[0]), "+v"(aq[1]), "+v"(aq[2]));
  f32x16 pq[3];
  feature_mfma(aq[0], aq[1], aq[2], wf, pq);
  const __amdgpu_buffer_rsrc_t rF = mkrs(P.ws + WS_KVF + (G * 4 + w) * 4096, 4096);
  {
    asm volatile("s_waitcnt vmcnt(0)" : "+v"(hp[0]), "+v"(hp[1]), "+v"(hp[2]), "+v"(hp[3])::"memory");
    float sv[4];
#pragma unroll
    for (int k = 0; k < 4; ++k) {
      float v = (__uint_as_float(hp[0][k]) + __uint_as_float(hp[1][k])) + (__uint_as_float(hp[2][k]) + __uint_as_float(hp[3][k]));
      const auto r = __builtin_amdgcn_permlane32_swap(__float_as_uint(v), __float_as_uint(v), false, false);
      const unsigned r0 = r[0], r1 = r[1];
      v = __uint_as_float(r0) + __uint_as_float(r1);
      const auto t = __builtin_amdgcn_permlane16_swap(__float_as_uint(v), __float_as_uint(v), false, false);
      const unsigned t0 = t[0], t1 = t[1];
      sv[k] = __uint_as_float(t0) + __uint_as_float(t1);
    }
    const u32x4 gr = {pk2(sv[0], sv[1]), tagl, pk2(sv[2], sv[3]), tagh};
    __builtin_amdgcn_raw_buffer_store_b128(gr, rF, g == 0 ? i * 16 : 0x7fffff00, c * 256, 16);
  }
  STAMP(9);
  const u32x4 sO = mksrd(P.wo, 640 * 1280);
  unsigned vO[5];
#pragma unroll
  for (int u = 0; u < 5; ++u) vO[u] = pk_[u] * 1280 + pu_[u] * 16;
  const unsigned so0 = ((w + sh) & 3) * 160 * 1280 + G * 160, lbo = w * 25600;
#define AOISSUE(N) dma16<(N) * 1024, ((N) / 5) * 40960>(sO, vO[(N) % 5], so0, lbo)
#define AOISSUE5(N) do { AOISSUE(N); AOISSUE(N + 1); AOISSUE(N + 2); AOISSUE(N + 3); AOISSUE(N + 4); } while (0)
  u32x4 phq[10];
  {
    float pv[20];
    u32x4 ph[5];
    AOISSUE5(0); AOISSUE5(5);
    head_vals<0>(pq, pv); dual_softmax(pv, ph);
#pragma unroll
    for (int u = 0; u < 5; ++u) { phq[u] = ph[u]; asm volatile("" : "+v"(phq[u])); }
    SCHED();
    AOISSUE5(10); AOISSUE5(15);
    head_vals<1>(pq, pv); dual_softmax(pv, ph);
#pragma unroll
    for (int u = 0; u < 5; ++u) { phq[5 + u] = ph[u]; asm volatile("" : "+v"(phq[5 + u])); }
    SCHED();
    AOISSUE5(20);
  }
  STAMP(10);
  {
    u32x4 gv[4];
    for (unsigned sp = 0; sp < SPIN_CAP; ++sp) {
      bool ok = true;
#pragma unroll
      for (int m = 0; m < 4; ++m) gv[m] = __builtin_amdgcn_raw_buffer_load_b128(rF, lane * 16, m * 1024, 16);
#pragma unroll
      for (int m = 0; m < 4; ++m) ok &= (lane + 64 * m >= 210) | ((gv[m][1] == tagl) & (gv[m][3] == tagh));
      if (__all(ok)) break;
      asm volatile("" ::: "memory");
    }
#pragma unroll
    for (int m = 0; m < 4; ++m) {
      const unsigned sl = lane + 64 * m, f4 = (sl * 3121u) >> 16, d = sl - 21 * f4;
      if (sl < 210) *(LAS u32x2*)(lds + A_KVT + w * 1680 + d * 80 + f4 * 8) = (u32x2){gv[m][0], gv[m][2]};
    }
  }
  bar();
  STAMP(11);
  const __amdgpu_buffer_rsrc_t rOb = mkrs(P.ws + WS_OB + c * 163840, 163840);
  const unsigned vol = w * 5120 + lane * 16;
  {
    const bool val = ((i32 >> 2) & 1) == h;
    const unsigned xo = i32 >> 3, e = i32 & 3, hb = A_KVT + 2 * h * 1680;
    const unsigned a0 = val ? hb + (e + 4 * xo) * 80 : (unsigned)A_ZERO, a1 = val ? a0 + 1680 : (unsigned)A_ZERO;
    const unsigned a2 = (val && !(xo & 1)) ? hb + (xo == 0 ? 16 + e : 20) * 80 : (unsigned)A_ZERO;
    const unsigned a3 = (val && (xo & 1)) ? hb + 1680 + (xo == 1 ? 16 + e : 20) * 80 : (unsigned)A_ZERO;
    bf16x8 fa_[4][5];
#pragma unroll
    for (int u = 0; u < 5; ++u) { fa_[0][u] = ldsr128(lds, a0 + 16 * u); fa_[1][u] = ldsr128(lds, a1 + 16 * u); fa_[2][u] = ldsr128(lds, a2 + 16 * u); fa_[3][u] = ldsr128(lds, a3 + 16 * u); }
    f32x16 oo[3];
#pragma unroll
    for (int u = 0; u < 5; ++u) {
      if (u == 0) { mfma32z(oo[0], fa_[0][u], __builtin_bit_cast(bf16x8, phq[u])); mfma32z(oo[1], fa_[1][u], __builtin_bit_cast(bf16x8, phq[5 + u])); mfma32z(oo[2], fa_[2][u], __builtin_bit_cast(bf16x8, phq[u])); }
      else { mfma32(oo[0], fa_[0][u], __builtin_bit_cast(bf16x8, phq[u])); mfma32(oo[1], fa_[1][u], __builtin_bit_cast(bf16x8, phq[5 + u])); mfma32(oo[2], fa_[2][u], __builtin_bit_cast(bf16x8, phq[u])); }
      mfma32(oo[2], fa_[3][u], __builtin_bit_cast(bf16x8, phq[5 + u]));
    }
    NOP12("+v"(oo[0]), "+v"(oo[1]), "+v"(oo[2]));
    const float r0 = __builtin_amdgcn_rcpf(oo[2][8] + 1e-6f), r1 = __builtin_amdgcn_rcpf(oo[2][12] + 1e-6f);
#pragma unroll
    for (int k = 0; k < 16; ++k) { oo[0][k] *= r0; oo[1][k] *= r1; }
#pragma unroll
    for (int k = 0; k < 4; ++k) { oo[2][k] *= r0; oo[2][4 + k] *= r1; }
    const u32x4 of[5] = {pk8(oo[0], 0), pk8(oo[0], 8), pk44(oo[2], 0, oo[1], 0), pk8(oo[1], 4), pk44(oo[1], 12, oo[2], 4)};
#pragma unroll
    for (int u = 0; u < 5; ++u) __builtin_amdgcn_raw_buffer_store_b128(of[u], rOb, vol, G * 20480 + u * 1024, 16);
  }
  STAMP(12);
  vmwait<0>(); bar();
  if (w == 0 && lane == 0) __hip_atomic_store((GAS u64*)(P.ws + WS_FA2 + (c * 8 + G) * 128), tag, __ATOMIC_RELAXED, __HIP_MEMORY_SCOPE_AGENT);
  STAMP(13);
  LBase<3, 2> aO;
  aO.v[0][0] = (40 * (g >> 1) + 2 * q) * 160 + 32 * (g & 1) + 8 * p;
  aO.v[0][1] = (40 * (g >> 1) + 8 * (g & 1) + 2 * q) * 160 + 128 + 8 * p;
  aO.v[0][2] = (40 * (g >> 1) + 48 * (g & 1) + 2 * q) * 160 + 128 + 8 * p;
  aO.fin();
  poll_flags(P.ws + WS_FA2 + c * 1024, 128, 8, lane, tag);
  STAMP(14);
  if (c == 0 && G == 0 && w == 0 && lane == 0) __hip_atomic_store((GAS u64*)(P.ws + WS_EPOCH_A), tag, __ATOMIC_RELAXED, __HIP_MEMORY_SCOPE_AGENT);
  u32x4 ofr[40];
#pragma unroll
  for (int S = 0; S < 40; ++S) ofr[S] = __builtin_amdgcn_raw_buffer_load_b128(rOb, vol, ((S / 5 + 2 * sh) & 7) * 20480 + (S % 5) * 1024, 16);
  f32x16 y[2];
  f32x4 z[2] = {(f32x4){0.f, 0.f, 0.f, 0.f}, (f32x4){0.f, 0.f, 0.f, 0.f}};
  bf16x8 fo[2][5];
#define AOOFF(S) ((80 * ((S) / 5) + 8 * ((S) % 5)) * 160)
#define A2RD(M, N) fo[(M) & 1][N] = (N) < 4 ? cat(ldstr(lds, aO.at<AOOFF(2 * (M) + ((N) >> 1 & 1)) + ((N) & 1) * 64>(0)), ldstr(lds, aO.at<AOOFF(2 * (M) + ((N) >> 1 & 1)) + ((N) & 1) * 64 + 160>(0))) \
                                            : cat(ldstr(lds, aO.at<AOOFF(2 * (M))>((2 * (M)) % 5 == 4 ? 2 : 1)), ldstr(lds, aO.at<AOOFF(2 * (M)) + 160>((2 * (M)) % 5 == 4 ? 2 : 1)))
#define A2RDX(M, N) do { if constexpr ((M) < 20) A2RD(M, N); } while (0)
  A2RD(0, 0); A2RD(0, 1); A2RD(0, 2); A2RD(0, 3); A2RD(0, 4);
  SCHED();
#define A2STEP(M) do { u32x4 f0 = ofr[2 * (M)], f1 = ofr[2 * (M) + 1]; \
    if ((M) == 0) mfma32z(y[0], __builtin_bit_cast(bf16x8, f0), fo[(M) & 1][0]); else mfma32(y[0], __builtin_bit_cast(bf16x8, f0), fo[(M) & 1][0]); \
    A2RDX((M) + 1, 0); SCHED(); \
    if ((M) == 0) mfma32z(y[1], __builtin_bit_cast(bf16x8, f0), fo[(M) & 1][1]); else mfma32(y[1], __builtin_bit_cast(bf16x8, f0), fo[(M) & 1][1]); \
    A2RDX((M) + 1, 1); A2RDX((M) + 1, 2); SCHED(); \
    mfma32(y[0], __builtin_bit_cast(bf16x8, f1), fo[(M) & 1][2]); \
    A2RDX((M) + 1, 3); A2RDX((M) + 1, 4); SCHED(); \
    mfma32(y[1], __builtin_bit_cast(bf16x8, f1), fo[(M) & 1][3]); \
    swap16x4(f0, f1); SCHED(); \
    mfma16x2(z[0], z[1], f0, f1, fo[(M) & 1][4]); SCHED(); } while (0)
#define A2STEP4(M) do { A2STEP(M); A2STEP(M + 1); A2STEP(M + 2); A2STEP(M + 3); } while (0)
  A2STEP4(0); A2STEP4(4); A2STEP4(8); A2STEP4(12); A2STEP4(16);
  STAMP(15);
  NOP12("+v"(y[0]), "+v"(y[1]), "+v"(z[0]), "+v"(z[1]));
  {
    const __amdgpu_buffer_rsrc_t rO = mkrs(P.out, 2048 * 5120);
    const unsigned so = (c * 128 + 32 * w) * 5120 + G * 320;
#pragma unroll
    for (int r = 0; r < 16; ++r) {
      const unsigned v = pk2(y[0][r], y[1][r]);
      __builtin_amdgcn_raw_buffer_store_b32(v << 16, rO, h * 20480 + i32 * 4, so + ((r & 3) + 8 * (r >> 2)) * 5120, 16);
      __builtin_amdgcn_raw_buffer_store_b32(v & 0xffff0000u, rO, h * 20480 + 128 + i32 * 4, so + ((r & 3) + 8 * (r >> 2)) * 5120, 16);
    }
#pragma unroll
    for (int r = 0; r < 4; ++r) {
      const unsigned v = pk2(z[0][r], z[1][r]);
      __builtin_amdgcn_raw_buffer_store_b32(v << 16, rO, g * 20480 + 256 + i * 4, so + r * 5120, 16);
      __builtin_amdgcn_raw_buffer_store_b32(v & 0xffff0000u, rO, g * 20480 + 256 + i * 4, so + (16 + r) * 5120, 16);
    }
  }
}

__global__ __launch_bounds__(256, 1) void fused_kernel(Params P) {
  __shared__ __attribute__((aligned(16))) char smem[163840];
  LAS char* lds = (LAS char*)smem;
  const int b = blockIdx.x, xcd = b & 7, loc = b >> 3, l = loc & 15;
  const int c = (xcd >> 1) * 4 + (l & 3), j = (xcd & 1) * 4 + (l >> 2);
  const int rot = ((((l & 3) + (l >> 2)) & 3) * 5) >> 1;
  const int w = __builtin_amdgcn_readfirstlane(threadIdx.x >> 6), lane = threadIdx.x & 63;
  STAMP(0);
  if (loc >= 16) chain_b(P, lds, c, j, rot, w, lane PROF_PASS);
  else chain_a(P, lds, c, j, rot, l >> 2, w, lane PROF_PASS);
  PROF_FLUSH();
}

extern "C" void kernel_launch(void* const* d_in, const int* in_sizes, int n_in, void* d_out, int out_size, void* d_ws,
                              size_t ws_size, hipStream_t stream) {
  Params P;
  P.Ax = (const u16*)d_in[0]; P.wqkv = (const u16*)d_in[1]; P.wphi = (const u16*)d_in[2]; P.wo = (const u16*)d_in[3];
  P.Bx = (const u16*)d_in[4]; P.lre = (const u16*)d_in[5]; P.lim = (const u16*)d_in[6]; P.bre = (const u16*)d_in[7];
  P.bim = (const u16*)d_in[8]; P.cre = (const u16*)d_in[9]; P.cim = (const u16*)d_in[10]; P.logdt = (const u16*)d_in[11];
  P.dsk = (const u16*)d_in[12]; P.out = (float*)d_out; P.ws = (char*)d_ws;
  fused_kernel<<<256, 256, 0, stream>>>(P);
}
```

```cpp
#include <hip/hip_runtime.h>
#include <hip/hip_bf16.h>
#include <stdint.h>

typedef unsigned short u16;
typedef unsigned long long u64;
typedef __bf16 bf16x8 __attribute__((ext_vector_type(8)));
typedef __bf16 bf16x4 __attribute__((ext_vector_type(4)));
typedef __bf16 bf16x2 __attribute__((ext_vector_type(2)));
typedef float f32x4 __attribute__((ext_vector_type(4)));
typedef float f32x2 __attribute__((ext_vector_type(2)));
typedef unsigned u32x4 __attribute__((ext_vector_type(4)));
typedef unsigned u32x2 __attribute__((ext_vector_type(2)));
#define LAS __attribute__((address_space(3)))
#define GAS __attribute__((address_space(1)))
#define CAS __attribute__((address_space(4)))
#define DI __device__ __forceinline__
#define SCHED() __builtin_amdgcn_sched_barrier(0)
typedef float f32x16 __attribute__((ext_vector_type(16)));

constexpr unsigned WS_EPOCH_A = 0, WS_EPOCH_B = 256;
constexpr unsigned WS_FA1 = 4096;
constexpr unsigned WS_FA2 = WS_FA1 + 512 * 128;
constexpr unsigned WS_FB2 = WS_FA2 + 128 * 128;
constexpr unsigned WS_GB1 = WS_FB2 + 128 * 128;
constexpr unsigned WS_KVF = WS_GB1 + 128 * 1280;
constexpr unsigned WS_KVP = WS_KVF + 32 * 4096;
constexpr unsigned WS_OB = WS_KVP + 512 * 4096;
constexpr unsigned WS_HB = WS_OB + 1024 * 2560;
constexpr unsigned WS_END = WS_HB + 16 * 320 * 1024;
constexpr unsigned SPIN_CAP = 1u << 17;

struct Params {
  const u16 *Ax, *wqkv, *wphi, *wo, *Bx, *lre, *lim, *bre, *bim, *cre, *cim, *logdt, *dsk;
  float* out;
  char* ws;
};

DI float bf2f(unsigned v) { return __uint_as_float(v << 16); }
DI float rbf(float f) { return (float)(__bf16)f; }
DI unsigned pk2(float a, float b) { f32x2 f = {a, b}; return __builtin_bit_cast(unsigned, __builtin_convertvector(f, bf16x2)); }
DI u32x4 mksrd(const void* p, unsigned bytes) {
  const u64 a = (u64)p;
  u32x4 r = {(unsigned)a, (unsigned)(a >> 32) & 0xffffu, bytes, 0x00020000u};
  return r;
}
DI __amdgpu_buffer_rsrc_t mkrs(const void* p, unsigned bytes) { return __builtin_amdgcn_make_buffer_rsrc((void*)p, 0, bytes, 0x00020000); }
template <int LI, int SI>
DI void dma16(u32x4 srd, unsigned vo, unsigned so, unsigned lb) {
  unsigned t;
  asm volatile("s_add_u32 m0, %[lb], %[li]\n\ts_add_u32 %[t], %[so], %[si]\n\tbuffer_load_dwordx4 %[vo], %[srd], %[t] offen lds"
               : [t] "=&s"(t) : [lb] "s"(lb), [li] "i"(LI), [so] "s"(so), [si] "i"(SI), [vo] "v"(vo), [srd] "s"(srd) : "memory", "scc");
}
DI void kadv(unsigned& k) { asm volatile("s_add_u32 %0, %0, 0x80\n\ts_cmp_eq_u32 %0, 0x500\n\ts_cselect_b32 %0, 0, %0" : "+s"(k)::"scc"); }
DI void mfma32(f32x16& c, bf16x8 a, bf16x8 b) { asm volatile("v_mfma_f32_32x32x16_bf16 %0, %1, %2, %0" : "+v"(c) : "v"(a), "v"(b)); }
DI void mfma32z(f32x16& c, bf16x8 a, bf16x8 b) { asm volatile("v_mfma_f32_32x32x16_bf16 %0, %1, %2, 0" : "=&v"(c) : "v"(a), "v"(b)); }
DI void mfma16(f32x4& c, bf16x8 a, bf16x8 b) { asm volatile("v_mfma_f32_16x16x32_bf16 %0, %1, %2, %0" : "+v"(c) : "v"(a), "v"(b)); }
DI void swap16x4(u32x4& a, u32x4& b) {
#pragma unroll
  for (int k = 0; k < 4; ++k) {
    const auto r = __builtin_amdgcn_permlane16_swap(a[k], b[k], false, false);
    const unsigned r0 = r[0], r1 = r[1];
    a[k] = r0; b[k] = r1;
  }
}
DI void mfma16x2(f32x4& c0, f32x4& c1, u32x4 a0, u32x4 a1, bf16x8 b) {
  asm volatile("s_nop 1\n\tv_mfma_f32_16x16x32_bf16 %0, %2, %4, %0\n\tv_mfma_f32_16x16x32_bf16 %1, %3, %4, %1" : "+v"(c0), "+v"(c1) : "v"(a0), "v"(a1), "v"(b));
}
template <int N, int NW> struct LBase {
  unsigned v[NW][N];
  DI void fin() {
#pragma unroll
    for (int k = 1; k < NW; ++k)
#pragma unroll
      for (int n = 0; n < N; ++n) { v[k][n] = v[0][n] + k * 65536; asm volatile("" : "+v"(v[k][n])); }
  }
  template <int OFF> DI unsigned at(int n) const { return v[OFF >> 16][n] + (OFF & 65535); }
};
template <int N> DI void vmwait() { asm volatile("s_waitcnt vmcnt(%0)" ::"i"(N) : "memory"); }
DI void bar() { asm volatile("s_waitcnt lgkmcnt(0)\n\ts_barrier" ::: "memory"); }
DI float dppx(float x) { return __builtin_bit_cast(float, __builtin_amdgcn_update_dpp(0, __builtin_bit_cast(int, x), 0xB1, 0xF, 0xF, true)); }
DI bf16x8 ldsr128(LAS char* l, unsigned off) { return *(LAS bf16x8*)(l + off); }
DI bf16x4 ldstr(LAS char* l, unsigned off) { return __builtin_amdgcn_ds_read_tr16_b64_v4bf16((LAS bf16x4*)(l + off)); }
DI bf16x8 cat(bf16x4 a, bf16x4 b) { return __builtin_shufflevector(a, b, 0, 1, 2, 3, 4, 5, 6, 7); }
#define STAMP(k) do {} while (0)
#define PROF_ARG
#define PROF_PASS
#define PROF_FLUSH() do {} while (0)

DI void scan_step(float& o0, float& o1, float& o2, float& o3, float& o4, float u0, float u1, float u2, float u3, float u4,
                  const float (&s)[5], const float (&a)[5], const float (&b)[5]) {
  asm("v_fma_f32 %0, %15, %10, %5\n\tv_fma_f32 %1, %16, %11, %6\n\tv_fma_f32 %2, %17, %12, %7\n\tv_fma_f32 %3, %18, %13, %8\n\tv_fma_f32 %4, %19, %14, %9\n\t"
      "v_fmac_f32_dpp %0, %10, %20 quad_perm:[1,0,3,2] row_mask:0xf bank_mask:0xf\n\t"
      "v_fmac_f32_dpp %1, %11, %21 quad_perm:[1,0,3,2] row_mask:0xf bank_mask:0xf\n\t"
      "v_fmac_f32_dpp %2, %12, %22 quad_perm:[1,0,3,2] row_mask:0xf bank_mask:0xf\n\t"
      "v_fmac_f32_dpp %3, %13, %23 quad_perm:[1,0,3,2] row_mask:0xf bank_mask:0xf\n\t"
      "v_fmac_f32_dpp %4, %14, %24 quad_perm:[1,0,3,2] row_mask:0xf bank_mask:0xf"
      : "=&v"(o0), "=&v"(o1), "=&v"(o2), "=&v"(o3), "=&v"(o4)
      : "v"(u0), "v"(u1), "v"(u2), "v"(u3), "v"(u4), "v"(s[0]), "v"(s[1]), "v"(s[2]), "v"(s[3]), "v"(s[4]),
        "v"(a[0]), "v"(a[1]), "v"(a[2]), "v"(a[3]), "v"(a[4]), "v"(b[0]), "v"(b[1]), "v"(b[2]), "v"(b[3]), "v"(b[4]));
}

DI void poll_flags(const char* base, unsigned stride, int n, int lane, u64 tag) {
  const GAS u64* fp = (const GAS u64*)(base + (unsigned)(lane < n ? lane : 0) * stride);
  for (unsigned sp = 0; sp < SPIN_CAP; ++sp) {
    const u64 v = __hip_atomic_load(fp, __ATOMIC_RELAXED, __HIP_MEMORY_SCOPE_AGENT);
    if (__all(v == tag)) break;
    __builtin_amdgcn_s_sleep(1);
  }
  asm volatile("" ::: "memory");
}

constexpr int RING = 36864, TB = 110592, T_PAR = TB, T_POW = TB + 2560, T_E = TB + 7680, T_L = TB + 12800, T_H = 161792;

template <int KC, int U>
DI void b1_dma(u32x4 sX, u32x4 sW, unsigned vx0, unsigned vx1, unsigned vw, unsigned sox, unsigned sow, unsigned lbx, unsigned lbw) {
  constexpr int slot = (KC % 3) * RING;
  if constexpr (U < 4) dma16<slot + U * 1024, U * 4 * 1280>(sX, (U & 1) ? vx1 : vx0, sox, lbx);
  else dma16<slot + 16384 + (U - 4) * 1024, (U - 4) * 10240>(sW, vw, sow, lbw);
}
constexpr int cbase(int ci, int part) {
  return ci == 3 ? part * 10240 : ci == 4 ? (part ? 98304 : 20480) : (ci == 5 || ci == 6) ? TB + (ci - 5) * 20480 + part * 10240
                                : 36864 + (ci >= 7 ? ci - 7 : ci) * 20480 + part * 10240;
}

constexpr int late_dma(int ks, int slot) {
  int n = 0;
  for (int k = 11; k <= 21; ++k)
    for (int sl = 0; sl < 2; ++sl) {
      const bool on = sl == 0 || k == 12 || k == 14 || k == 16 || k == 18;
      if (k == ks && sl == slot) return on ? n : -1;
      if (on) ++n;
    }
  return -1;
}
struct Disc { float ld, a, b, dt, e, y, n, r, z, sp, cp, lr, li, cr, ci, t0, t1, t2, qr, qi, pr, pi; };
#define PIN1(x) asm volatile("" : "+v"(x))
#define PIN2(x, y) asm volatile("" : "+v"(x), "+v"(y))
#define PIN3(x, y, z) asm volatile("" : "+v"(x), "+v"(y), "+v"(z))
template <int K>
DI void disc_slice(Disc& D, LAS char* lds, int pl) {
  constexpr float L2E = 1.4426950408889634f;
  if constexpr (K == 0) { D.dt = rbf(__builtin_amdgcn_exp2f(D.ld * L2E)); PIN1(D.dt); }
  if constexpr (K == 1) { D.e = __builtin_amdgcn_exp2f((D.dt * D.a) * L2E); D.y = D.dt * D.b; PIN2(D.e, D.y); }
  if constexpr (K == 2) { D.n = __builtin_rintf(D.y * 0.6366197723675814f); D.r = __builtin_fmaf(D.n, -1.5703125f, D.y); PIN2(D.n, D.r); }
  if constexpr (K == 3) { D.r = __builtin_fmaf(D.n, -4.837512969970703125e-4f, D.r); D.r = __builtin_fmaf(D.n, -7.54978995489188216e-8f, D.r); D.z = D.r * D.r; PIN2(D.r, D.z); }
  if constexpr (K == 4) { D.sp = __builtin_fmaf(__builtin_fmaf(D.z, -1.9515295891e-4f, 8.3321608736e-3f), D.z, -1.6666654611e-1f); D.t0 = D.r * D.z; PIN2(D.sp, D.t0); }
  if constexpr (K == 5) { D.sp = __builtin_fmaf(D.sp, D.t0, D.r); D.cp = __builtin_fmaf(__builtin_fmaf(D.z, 2.443315711809948e-5f, -1.388731625493765e-3f), D.z, 4.166664568298827e-2f); PIN2(D.sp, D.cp); }
  if constexpr (K == 6) { D.t0 = D.z * D.z; D.t1 = __builtin_fmaf(D.z, -0.5f, 1.f); D.cp = __builtin_fmaf(D.cp, D.t0, D.t1); PIN1(D.cp); }
  if constexpr (K == 7) { const int q = (int)D.n; D.t2 = __int_as_float(q); D.t0 = (q & 1) ? D.cp : D.sp; D.t1 = (q & 1) ? D.sp : D.cp; PIN3(D.t0, D.t1, D.t2); }
  if constexpr (K == 8) { const unsigned q = __float_as_uint(D.t2); D.t0 = __uint_as_float(__float_as_uint(D.t0) ^ ((q & 2u) << 30)); D.t2 = __uint_as_float(((q + 1u) & 2u) << 30); PIN2(D.t0, D.t2); }
  if constexpr (K == 9) { D.t1 = __uint_as_float(__float_as_uint(D.t1) ^ __float_as_uint(D.t2)); D.li = D.e * D.t0; D.lr = D.e * D.t1; PIN2(D.lr, D.li); }
  if constexpr (K == 10) { D.t0 = __builtin_amdgcn_rcpf(fmaxf(fabsf(D.a), fabsf(D.b))); D.t2 = D.lr - 1.f; PIN2(D.t0, D.t2); }
  if constexpr (K == 11) { D.a *= D.t0; D.b *= D.t0; D.t1 = D.a * D.a; PIN3(D.a, D.b, D.t1); }
  if constexpr (K == 12) { D.t1 = __builtin_amdgcn_rcpf(__builtin_fmaf(D.b, D.b, D.t1)); D.cr = D.t2 * D.a; PIN2(D.t1, D.cr); }
  if constexpr (K == 13) { D.t1 *= D.t0; D.cr = __builtin_fmaf(D.li, D.b, D.cr); D.ci = D.li * D.a; PIN3(D.t1, D.cr, D.ci); }
  if constexpr (K == 14) { D.ci = __builtin_fmaf(-D.t2, D.b, D.ci) * D.t1; D.cr *= D.t1; PIN2(D.cr, D.ci); }
  if constexpr (K == 15) { *(LAS f32x4*)(lds + T_PAR + pl * 32) = (f32x4){D.lr, D.li, D.cr, D.ci}; D.qr = D.lr; D.qi = D.li; }
  if constexpr (K >= 16 && K < 20) { const float t = __builtin_fmaf(D.qr, D.qr, -D.qi * D.qi); D.qi = (D.qr + D.qr) * D.qi; D.qr = t; PIN2(D.qr, D.qi); }
  if constexpr (K == 20) { *(LAS f32x2*)(lds + T_POW + pl * 8) = (f32x2){1.f, 0.f}; D.pr = D.qr; D.pi = D.qi; }
  if constexpr (K >= 21 && K < 28) {
    *(LAS f32x2*)(lds + T_POW + ((K - 20) * 80 + pl) * 8) = (f32x2){D.pr, D.pi};
    const float t = __builtin_fmaf(D.pr, D.qr, -D.pi * D.qi); D.pi = __builtin_fmaf(D.pr, D.qi, D.pi * D.qr); D.pr = t; PIN2(D.pr, D.pi);
  }
  if constexpr (K == 28) *(LAS f32x4*)(lds + T_PAR + pl * 32 + 16) = (f32x4){D.pr, D.pi, D.qr, D.qi};
}

#define CODE_PREFETCH(BEG, END) \
  unsigned pf_dummy; \
  { u64 pc_; unsigned sz_; \
    asm volatile("s_getpc_b64 %0\n" BEG ":\n\ts_mov_b32 %1, " END "-" BEG : "=s"(pc_), "=s"(sz_)); \
    const u32x4 sp_ = mksrd((const void*)pc_, sz_); const unsigned vo_ = (w * 64 + lane) * 128; \
    asm volatile("buffer_load_dword %0, %1, %2, 0 offen" : "=&v"(pf_dummy) : "v"(vo_), "s"(sp_) : "memory"); }
#define CODE_END(END) asm volatile(END ":" ::: "memory")
DI void chain_b(const Params& P, LAS char* lds, int c, int j, int rot, int w, int lane PROF_ARG) {
  CODE_PREFETCH(".Lcb_beg", ".Lcb_end");
  const int g = lane >> 4, i = lane & 15, h = lane >> 5, i32 = lane & 31, e3 = lane >> 3, pos = lane & 7, wr = w >> 1, wc = w & 1;
  const u32x4 sX = mksrd(P.Bx, 2048 * 1280), sW = mksrd(wr ? P.bim : P.bre, 640 * 1280);
  const unsigned vxr = (16 * (e3 >> 2) + (e3 & 3)) * 1280;
  const unsigned vx0 = vxr + ((pos ^ (e3 >> 1)) << 4), vx1 = vxr + ((pos ^ (4 + (e3 >> 1))) << 4);
  const unsigned vw = e3 * 1280 + ((pos ^ ((e3 & 6) | wr)) << 4);
  const unsigned sx0 = (c * 128 + 32 * w) * 1280, sw0 = (j * 80 + 40 * wc) * 1280;
  const unsigned lbx = w * 4096, lbw = w * 5120;
  unsigned kc1 = rot * 128;
#define B1ISSUE(KC, U) do { b1_dma<KC, U>(sX, sW, vx0, vx1, vw, sx0 + kc1, sw0 + kc1, lbx, lbw); if ((U) == 8) kadv(kc1); } while (0)
#define B1ALL(KC) do { B1ISSUE(KC, 0); B1ISSUE(KC, 4); B1ISSUE(KC, 5); B1ISSUE(KC, 1); B1ISSUE(KC, 6); B1ISSUE(KC, 2); B1ISSUE(KC, 7); B1ISSUE(KC, 3); B1ISSUE(KC, 8); } while (0)
  const int plc = 20 * w + (lane < 20 ? lane : 0);
  unsigned in0, in1, in2;
  { u32x4 t_ = sX; asm volatile("" : "+s"(t_)); STAMP(1); }
  B1ISSUE(0, 0);
  STAMP(2);
  B1ISSUE(0, 4); B1ISSUE(0, 5); B1ISSUE(0, 1); B1ISSUE(0, 6);
  STAMP(3);
  B1ISSUE(0, 2); B1ISSUE(0, 7); B1ISSUE(0, 3); B1ISSUE(0, 8);
  STAMP(4);
  {
    const unsigned po = (j * 80 + plc) * 2;
    asm volatile("global_load_ushort %0, %3, %4\n\tglobal_load_ushort %1, %3, %5\n\tglobal_load_ushort %2, %3, %6"
                 : "=&v"(in0), "=&v"(in1), "=&v"(in2) : "v"(po), "s"(P.logdt), "s"(P.lre), "s"(P.lim) : "memory");
  }
  u64 tag = *(const CAS u64*)(P.ws + WS_EPOCH_B);
  const u32x4 sC = mksrd(wr ? P.cim : P.cre, 640 * 1280);
  unsigned vC[5];
#pragma unroll
  for (int u = 0; u < 5; ++u) {
    const int R = 8 * (5 * wc + u) + e3, d = R < 64 ? 2 * (R & 31) + (R >> 5) : R;
    vC[u] = (j * 80 + d) * 1280 + ((pos ^ ((R >> 1) & 7)) << 4);
  }
  const unsigned lb4 = (wr ? 98304 : 20480) + wc * 5120;
  unsigned kc2 = rot * 128;
#define B2ISSUE(CI, U) do { dma16<((CI) == 4 ? 0 : cbase(CI, 0)) + (U) * 1024, 0>(sC, vC[U], kc2, (CI) == 4 ? lb4 : lbw); if ((U) == 4) kadv(kc2); } while (0)
  f32x16 acc[5];
  const unsigned ra = (w * 32 + i32) * 128, rb = 16384 + ((i32 & 1) * 80 + (i32 >> 1)) * 128;
  const unsigned ma = (i32 >> 1) & 7, mb = (((i32 >> 2) & 3) << 1) | (i32 & 1);
  LBase<4, 2> aA, aB;
#pragma unroll
  for (int s = 0; s < 4; ++s) { aA.v[0][s] = ra + (((2 * s + h) ^ ma) << 4); aB.v[0][s] = rb + (((2 * s + h) ^ mb) << 4); }
  aA.fin(); aB.fin();
  bf16x8 fa[2][2], fb[2][2][5];
#define B1RD(KC, HF, Q) do { constexpr int sl = ((KC) % 3) * RING, s2 = (Q) / 6, q6 = (Q) % 6; \
    if (q6 == 0) fa[HF][s2] = ldsr128(lds, aA.at<sl>(2 * (HF) + s2)); else fb[HF][s2][q6 - 1] = ldsr128(lds, aB.at<sl + (q6 > 0 ? q6 - 1 : 0) * 2048>(2 * (HF) + s2)); } while (0)
  SCHED();
  B1ALL(1);
  STAMP(5);
  asm volatile("s_waitcnt vmcnt(9)" : "+v"(in0), "+v"(in1), "+v"(in2), "+v"(pf_dummy)::"memory");
  bar();
  STAMP(6);
  asm volatile("" : "+s"(tag));
  tag += 0x9E3779B97F4A7C15ull;
  const unsigned tagl = (unsigned)tag, tagh = (unsigned)(tag >> 32);
  Disc D; D.ld = bf2f(in0); D.a = bf2f(in1); D.b = bf2f(in2);
#define B1RD2(KC, HF, X) do { B1RD(KC, HF, 2 * (X)); B1RD(KC, HF, 2 * (X) + 1); } while (0)
  B1RD2(0, 0, 0); B1RD2(0, 0, 1); B1RD2(0, 0, 2); B1RD2(0, 0, 3); B1RD2(0, 0, 4); B1RD2(0, 0, 5);
  SCHED();
#define B1GAP(KC, HF, X, RKC, RD, ISS) do { \
    if ((KC) == 0 && (HF) == 0 && (X) < 5) mfma32z(acc[(X) % 5], fa[HF][(X) / 5], fb[HF][(X) / 5][(X) % 5]); \
    else mfma32(acc[(X) % 5], fa[HF][(X) / 5], fb[HF][(X) / 5][(X) % 5]); \
    if (RD) { if ((X) == 0) B1RD2(RKC, 1 - (HF), 0); if ((X) == 1) B1RD2(RKC, 1 - (HF), 1); if ((X) == 3) B1RD2(RKC, 1 - (HF), 2); \
              if ((X) == 4) B1RD2(RKC, 1 - (HF), 3); if ((X) == 6) B1RD2(RKC, 1 - (HF), 4); if ((X) == 7) B1RD2(RKC, 1 - (HF), 5); } \
    if ((KC) == 0 && (HF) == 0) { if ((X) >= 1) B1ISSUE(2, ((X) >= 1 ? (X) - 1 : 0)); } \
    else { if ((X) == 2) { ISS(0); } if ((X) == 5) { ISS(1); } if ((X) == 8) { ISS(2); } if ((X) == 9) { ISS(3); } if ((X) == 0) { ISS(4); } } \
    if ((KC) >= 1 && (KC) <= 3) { constexpr int ds_ = ((KC) - 1) * 12 + (HF) * 6 + ((X) == 0 ? 0 : (X) == 1 ? 1 : (X) == 3 ? 2 : (X) == 4 ? 3 : (X) == 6 ? 4 : (X) == 7 ? 5 : -100); \
      if constexpr (ds_ >= 0 && ds_ <= 28) disc_slice<ds_>(D, lds, plc); } \
    SCHED(); } while (0)
#define B1HALF(KC, HF, RKC, RD, ISS) do { B1GAP(KC, HF, 0, RKC, RD, ISS); B1GAP(KC, HF, 1, RKC, RD, ISS); B1GAP(KC, HF, 2, RKC, RD, ISS); B1GAP(KC, HF, 3, RKC, RD, ISS); \
    B1GAP(KC, HF, 4, RKC, RD, ISS); B1GAP(KC, HF, 5, RKC, RD, ISS); B1GAP(KC, HF, 6, RKC, RD, ISS); B1GAP(KC, HF, 7, RKC, RD, ISS); B1GAP(KC, HF, 8, RKC, RD, ISS); B1GAP(KC, HF, 9, RKC, RD, ISS); } while (0)
#define B1STEP(KC, ISSA, VM, ISSB) do { B1HALF(KC, 0, KC, true, ISSA); if ((KC) < 9) { vmwait<VM>(); bar(); } B1HALF(KC, 1, ((KC) < 9 ? (KC) + 1 : 0), ((KC) < 9), ISSB); } while (0)
#define IA(KC, n) do { if ((n) < 4) B1ISSUE((KC) + 2, 5 + ((n) & 3)); } while (0)
#define IB(KC, n) B1ISSUE((KC) + 3, ((n) + 1) % 5)
#define IA0(n) IA(0, n)
#define IA1(n) IA(1, n)
#define IA2(n) IA(2, n)
#define IA3(n) IA(3, n)
#define IA4(n) IA(4, n)
#define IA5(n) IA(5, n)
#define IA6(n) IA(6, n)
#define IA7(n) IA(7, n)
#define IB0(n) IB(0, n)
#define IB1(n) IB(1, n)
#define IB2(n) IB(2, n)
#define IB3(n) IB(3, n)
#define IB4(n) IB(4, n)
#define IB5(n) IB(5, n)
#define IB6(n) IB(6, n)
#define IC0(n) B2ISSUE(0, ((n) + 1) % 5)
#define IC1(n) B2ISSUE(1, ((n) + 1) % 5)
#define IC2(n) B2ISSUE(2, ((n) + 1) % 5)
#define INONE(n) do {} while (0)
  B1STEP(0, IA0, 9, IB0); B1STEP(1, IA1, 9, IB1); B1STEP(2, IA2, 9, IB2); B1STEP(3, IA3, 9, IB3); B1STEP(4, IA4, 9, IB4);
  B1STEP(5, IA5, 9, IB5); B1STEP(6, IA6, 9, IB6); B1STEP(7, IA7, 9, IC0); B1STEP(8, INONE, 5, IC1); B1STEP(9, IC2, 0, INONE);
  STAMP(7);
  const int part = lane & 1, sub = 2 * w + h, pl0 = i32 >> 1;
  float la[5], lb[5], ko[5], kx[5], k1[5], k2[5], cfr[5], cfi[5];
#pragma unroll
  for (int n = 0; n < 5; ++n) {
    const f32x4 pp = *(LAS f32x4*)(lds + T_PAR + (pl0 + 16 * n) * 32);
    la[n] = pp[0]; lb[n] = part ? pp[1] : -pp[1];
    cfr[n] = pp[2]; cfi[n] = pp[3];
  }
  asm volatile("s_nop 7\n\ts_nop 3" : "+v"(acc[0]), "+v"(acc[1]), "+v"(acc[2]), "+v"(acc[3]), "+v"(acc[4]));
  {
    float s[5] = {acc[0][0], acc[1][0], acc[2][0], acc[3][0], acc[4][0]};
#pragma unroll
    for (int t = 1; t < 16; ++t) {
      float o[5];
      scan_step(o[0], o[1], o[2], o[3], o[4], acc[0][t], acc[1][t], acc[2][t], acc[3][t], acc[4][t], s, la, lb);
#pragma unroll
      for (int n = 0; n < 5; ++n) s[n] = o[n];
    }
#pragma unroll
    for (int n = 0; n < 5; ++n) *(LAS float*)(lds + T_E + (sub * 160 + n * 32 + i32) * 4) = s[n];
  }
  bar();
  STAMP(8);
  const __amdgpu_buffer_rsrc_t rG = mkrs(P.ws + WS_GB1, 128 * 1280);
  unsigned gme = (j * 16 + c) * 1280;
  asm volatile("" : "+s"(gme));
  const f32x4 pq = *(LAS f32x4*)(lds + T_PAR + plc * 32 + 16);
  {
    float hr = 0.f, hi = 0.f;
    f32x2 e[8];
#pragma unroll
    for (int k = 0; k < 8; ++k) e[k] = *(LAS f32x2*)(lds + T_E + (k * 80 + plc) * 8);
    f32x2 lp[8];
#pragma unroll
    for (int k = 0; k < 8; ++k) {
      lp[k] = (f32x2){hr, hi};
      const float t = __builtin_fmaf(pq[2], hr, __builtin_fmaf(-pq[3], hi, e[k][0])); hi = __builtin_fmaf(pq[2], hi, __builtin_fmaf(pq[3], hr, e[k][1])); hr = t;
    }
    const u32x4 gr = {__float_as_uint(hr), tagl, __float_as_uint(hi), tagh};
    if (lane < 20) __builtin_amdgcn_raw_buffer_store_b128(gr, rG, plc * 16, gme, 16);
#pragma unroll
    for (int k = 0; k < 8; ++k) *(LAS f32x2*)(lds + T_L + (k * 80 + plc) * 8) = lp[k];
  }
  STAMP(9);
#pragma unroll
  for (int n = 0; n < 5; ++n) {
    const f32x2 pw = *(LAS f32x2*)(lds + T_POW + (sub * 80 + pl0 + 16 * n) * 8);
    ko[n] = part ? -2.f * cfr[n] : 2.f * cfr[n]; kx[n] = -2.f * cfi[n];
    const float wr_ = 2.f * (cfr[n] * pw[0] - cfi[n] * pw[1]), wi_ = -2.f * (cfr[n] * pw[1] + cfi[n] * pw[0]);
    k1[n] = part ? wi_ : wr_; k2[n] = part ? -wr_ : wi_;
    PIN2(k1[n], k2[n]);
  }
#pragma unroll
  for (int x = 0; x < 20; ++x) {
    const int n = x >> 2, q = (x & 3) * 4;
    float u0 = acc[n][q], u1 = acc[n][q + 1], u2 = acc[n][q + 2], u3 = acc[n][q + 3], t0, t1, t2, t3;
    asm volatile("s_nop 1\n\tv_mul_f32_dpp %4, %0, %8 quad_perm:[1,0,3,2] row_mask:0xf bank_mask:0xf\n\tv_mul_f32_dpp %5, %1, %8 quad_perm:[1,0,3,2] row_mask:0xf bank_mask:0xf\n\t"
                 "v_mul_f32_dpp %6, %2, %8 quad_perm:[1,0,3,2] row_mask:0xf bank_mask:0xf\n\tv_mul_f32_dpp %7, %3, %8 quad_perm:[1,0,3,2] row_mask:0xf bank_mask:0xf\n\t"
                 "v_fma_f32 %0, %0, %9, %4\n\tv_fma_f32 %1, %1, %9, %5\n\tv_fma_f32 %2, %2, %9, %6\n\tv_fma_f32 %3, %3, %9, %7"
                 : "+v"(u0), "+v"(u1), "+v"(u2), "+v"(u3), "=&v"(t0), "=&v"(t1), "=&v"(t2), "=&v"(t3) : "v"(kx[n]), "v"(ko[n]));
    acc[n][q] = u0; acc[n][q + 1] = u1; acc[n][q + 2] = u2; acc[n][q + 3] = u3;
  }
  STAMP(10);
  {
    float hr = 0.f, hi = 0.f;
    u32x4 gv[15];
    for (unsigned sp = 0; sp < SPIN_CAP; ++sp) {
      bool ok = true;
#pragma unroll
      for (int k = 0; k < 15; ++k)
        gv[k] = __builtin_amdgcn_raw_buffer_load_b128(rG, plc * 16, c - 15 + k >= 0 ? gme - (15 - k) * 1280 : 0x7fffff00, 16);
#pragma unroll
      for (int k = 0; k < 15; ++k) ok &= (c - 15 + k < 0) | ((gv[k][1] == tagl) & (gv[k][3] == tagh));
      if (__all(ok)) break;
      asm volatile("" ::: "memory");
    }
    STAMP(11);
#pragma unroll
    for (int k = 0; k < 15; ++k) {
      const float t = __builtin_fmaf(pq[0], hr, __builtin_fmaf(-pq[1], hi, __uint_as_float(gv[k][0])));
      hi = __builtin_fmaf(pq[0], hi, __builtin_fmaf(pq[1], hr, __uint_as_float(gv[k][2]))); hr = t;
    }
    *(LAS f32x2*)(lds + T_H + plc * 8) = (f32x2){hr, hi};
  }
  bar();
  STAMP(12);
  float s[5];
#pragma unroll
  for (int n = 0; n < 5; ++n) {
    const f32x2 L = *(LAS f32x2*)(lds + T_L + (sub * 80 + pl0 + 16 * n) * 8);
    const f32x2 H = *(LAS f32x2*)(lds + T_H + (pl0 + 16 * n) * 8);
    const float lg = part ? -2.f * (cfr[n] * L[1] + cfi[n] * L[0]) : 2.f * (cfr[n] * L[0] - cfi[n] * L[1]);
    s[n] = lg + k1[n] * H[0] + k2[n] * H[1];
    lb[n] = -lb[n];
  }
  bar();
#pragma unroll
  for (int t = 0; t < 16; ++t) {
    float o[5];
    scan_step(o[0], o[1], o[2], o[3], o[4], acc[0][t], acc[1][t], acc[2][t], acc[3][t], acc[4][t], s, la, lb);
#pragma unroll
    for (int n = 0; n < 5; ++n) { s[n] = o[n]; acc[n][t] = o[n]; }
    PIN1(s[0]);
    if (t == 0) B2ISSUE(3, 0); if (t == 1) B2ISSUE(3, 1); if (t == 2) B2ISSUE(3, 2); if (t == 3) B2ISSUE(3, 3); if (t == 4) B2ISSUE(3, 4);
    if (t == 5) B2ISSUE(4, 0); if (t == 6) B2ISSUE(4, 1); if (t == 7) B2ISSUE(4, 2); if (t == 8) B2ISSUE(4, 3); if (t == 9) B2ISSUE(4, 4);
    SCHED();
  }
  const unsigned trw = TB + w * 12800 + i32 * 80 + h * 32;
#pragma unroll
  for (int n = 0; n < 5; ++n) {
#pragma unroll
    for (int hh = 0; hh < 2; ++hh) {
      const u32x4 v = {pk2(acc[n][8 * hh], acc[n][8 * hh + 1]), pk2(acc[n][8 * hh + 2], acc[n][8 * hh + 3]),
                       pk2(acc[n][8 * hh + 4], acc[n][8 * hh + 5]), pk2(acc[n][8 * hh + 6], acc[n][8 * hh + 7])};
      *(LAS u32x4*)(lds + trw + n * 2560 + hh * 16) = v;
    }
  }
  const __amdgpu_buffer_rsrc_t rH = mkrs(P.ws + WS_HB + c * 327680, 327680);
  const unsigned vhl = w * 1024 + lane * 16;
  {
    const unsigned trr = TB + w * 12800 + (16 * h + 2 * (i >> 2)) * 80 + (g & 1) * 32 + (i & 3) * 8;
#pragma unroll
    for (int n = 0; n < 5; ++n)
#pragma unroll
      for (int pt = 0; pt < 2; ++pt) {
        const bf16x8 v = cat(ldstr(lds, trr + n * 2560 + pt * 80), ldstr(lds, trr + n * 2560 + pt * 80 + 640));
        __builtin_amdgcn_raw_buffer_store_b128(__builtin_bit_cast(u32x4, v), rH, vhl, (pt * 40 + j * 5 + n) * 4096, 16);
      }
  }
  STAMP(13);
  vmwait<0>(); bar();
  if (w == 0 && lane == 0) __hip_atomic_store((GAS u64*)(P.ws + WS_FB2 + (c * 8 + j) * 128), tag, __ATOMIC_RELAXED, __HIP_MEMORY_SCOPE_AGENT);
  STAMP(14);
  f32x16 y[2];
  f32x4 z[2];
  {
    const unsigned d2 = *(const unsigned*)(P.dsk + j * 80 + 2 * i32);
    const float d0 = bf2f(d2 & 0xffffu), d1 = bf2f(d2 >> 16), d4 = bf2f(P.dsk[j * 80 + 64 + i]);
    const u16* xr = P.Bx + (c * 128 + 32 * w) * 640 + j * 80;
    unsigned xa[16], xb[8];
#pragma unroll
    for (int r = 0; r < 16; ++r) xa[r] = *(const unsigned*)(xr + ((r & 3) + 8 * (r >> 2) + 4 * h) * 640 + 2 * i32);
#pragma unroll
    for (int r = 0; r < 8; ++r) xb[r] = xr[(16 * (r >> 2) + 4 * g + (r & 3)) * 640 + 64 + i];
    SCHED();
    B2ISSUE(5, 0); B2ISSUE(5, 1); B2ISSUE(5, 2); B2ISSUE(5, 3); B2ISSUE(5, 4);
    B2ISSUE(6, 0); B2ISSUE(6, 1); B2ISSUE(6, 2); B2ISSUE(6, 3); B2ISSUE(6, 4);
#pragma unroll
    for (int r = 0; r < 16; ++r) { y[0][r] = rbf(bf2f(xa[r] & 0xffffu) * d0); y[1][r] = rbf(bf2f(xa[r] >> 16) * d1); }
#pragma unroll
    for (int r = 0; r < 8; ++r) z[r >> 2][r & 3] = rbf(bf2f(xb[r]) * d4);
    asm volatile("" : "+v"(y[0]), "+v"(y[1]), "+v"(z[0]), "+v"(z[1]));
  }
  STAMP(15);
  poll_flags(P.ws + WS_FB2 + c * 1024, 128, 8, lane, tag);
  STAMP(16);
  if (c == 15 && j == 0 && w == 0 && lane == 0) __hip_atomic_store((GAS u64*)(P.ws + WS_EPOCH_B), tag, __ATOMIC_RELAXED, __HIP_MEMORY_SCOPE_AGENT);
#define HAUX 16
#define XPD 6
  constexpr int PD = XPD;
  u32x4 hf_[PD][2];
  unsigned kc3 = rot * 128;
#define B2HLD(KS) do { const unsigned so_ = ((((KS) >> 1) & 1) * 40 + ((KS) & 1) * 2) * 4096 + kc3 * 128; \
                       hf_[(KS) % PD][0] = __builtin_amdgcn_raw_buffer_load_b128(rH, vhl, so_, HAUX); \
                       hf_[(KS) % PD][1] = __builtin_amdgcn_raw_buffer_load_b128(rH, vhl, so_ + 4096, HAUX); if (((KS) & 3) == 3) kadv(kc3); } while (0)
#pragma unroll
  for (int ks = 0; ks < PD - 1; ++ks) B2HLD(ks);
  LBase<6, 3> aC;
#pragma unroll
  for (int s = 0; s < 4; ++s) aC.v[0][s] = i32 * 128 + (((2 * s + h) ^ ma) << 4);
#pragma unroll
  for (int s = 0; s < 2; ++s) aC.v[0][4 + s] = (64 + i) * 128 + (((4 * s + 2 * (g & 1) + (g >> 1)) ^ (i >> 1)) << 4);
  aC.fin();
  bf16x8 fc[2][5];
#define B2RD(KS, N) fc[(KS) & 1][N] = ldsr128(lds, aC.at<cbase((KS) >> 2, ((KS) >> 1) & 1) + ((N) < 4 ? ((N) & 1) * 4096 : 0)>((N) < 4 ? 2 * ((KS) & 1) + ((N) >> 1) : 4 + ((KS) & 1)))
  B2RD(0, 0); B2RD(0, 1); B2RD(0, 2); B2RD(0, 3); B2RD(0, 4);
  SCHED();
#define B2LATE(N) do { if constexpr ((N) >= 0) B2ISSUE(7 + ((N) < 0 ? 0 : (N)) / 5, ((N) < 0 ? 0 : (N)) % 5); } while (0)
#define X2 0
#define B2HLD1(KS, Q) do { if constexpr ((KS) < 40 && !(X2 & 1)) { const unsigned so_ = ((((KS) >> 1) & 1) * 40 + ((KS) & 1) * 2 + (Q)) * 4096 + kc3 * 128; \
                       hf_[(KS) % PD][Q] = __builtin_amdgcn_raw_buffer_load_b128(rH, vhl, so_, HAUX); if (((KS) & 3) == 3 && (Q) == 1) kadv(kc3); } } while (0)
#define B2RDX(KS, N) do { if constexpr ((KS) < 40 && !(X2 & 2)) B2RD(KS, N); } while (0)
#define B2STEP(KS) do { \
    if ((KS) == 27) vmwait<10>(); \
    if ((KS) == 11 || (KS) == 27) bar();   \
    u32x4 f0 = hf_[(KS) % PD][0], f1 = hf_[(KS) % PD][1]; \
    mfma32(y[0], __builtin_bit_cast(bf16x8, f0), fc[(KS) & 1][0]); \
    B2RDX((KS) + 1, 0); B2RDX((KS) + 1, 1); B2RDX((KS) + 1, 2); \
    SCHED(); \
    mfma32(y[1], __builtin_bit_cast(bf16x8, f0), fc[(KS) & 1][1]); \
    B2HLD1((KS) + PD - 1, 0); \
    SCHED(); \
    mfma32(y[0], __builtin_bit_cast(bf16x8, f1), fc[(KS) & 1][2]); \
    B2HLD1((KS) + PD - 1, 1); \
    SCHED(); \
    mfma32(y[1], __builtin_bit_cast(bf16x8, f1), fc[(KS) & 1][3]); \
    if constexpr (!(X2 & 4)) swap16x4(f0, f1); \
    B2RDX((KS) + 1, 3); B2RDX((KS) + 1, 4); \
    SCHED(); \
    mfma16x2(z[0], z[1], f0, f1, fc[(KS) & 1][4]); \
    B2LATE(late_dma(KS, 0)); B2LATE(late_dma(KS, 1)); \
    SCHED(); } while (0)
#define B2STEP4(K) do { B2STEP(K); B2STEP(K + 1); B2STEP(K + 2); B2STEP(K + 3); } while (0)
  B2STEP4(0); B2STEP4(4); B2STEP4(8); B2STEP4(12); B2STEP4(16); B2STEP4(20); B2STEP4(24); B2STEP4(28); B2STEP4(32); B2STEP4(36);
  STAMP(17);
  asm volatile("s_nop 7\n\ts_nop 3" : "+v"(y[0]), "+v"(y[1]), "+v"(z[0]), "+v"(z[1]));
  {
    const __amdgpu_buffer_rsrc_t rO = mkrs(P.out, 2048 * 5120);
    const unsigned so = (c * 128 + 32 * w) * 5120 + (640 + j * 80) * 4;
#pragma unroll
    for (int r = 0; r < 16; ++r) {
      const u32x2 v = {__float_as_uint(y[0][r]), __float_as_uint(y[1][r])};
      __builtin_amdgcn_raw_buffer_store_b64(v, rO, h * 20480 + i32 * 8, so + ((r & 3) + 8 * (r >> 2)) * 5120, 16);
    }
#pragma unroll
    for (int r = 0; r < 8; ++r)
      __builtin_amdgcn_raw_buffer_store_b32(__float_as_uint(z[r >> 2][r & 3]), rO, g * 20480 + 256 + i * 4, so + (16 * (r >> 2) + (r & 3)) * 5120, 16);
  }
  CODE_END(".Lcb_end");
}

constexpr int RINGA = 36928, A_Q0 = RINGA, A_R1 = 118848, A_WPHI = 151616, A_KVT = 154816, A_ZERO = 161536, A_ONES = 163072;
constexpr int qbase(int qc) { return qc < 4 ? A_Q0 + qc * 20480 : 0; }
DI void mfma32a(f32x16& c, bf16x8 a, bf16x8 b) { asm volatile("v_mfma_f32_32x32x16_bf16 %0, %1, %2, %0" : "+v"(c) : "v"(a), "a"(b)); }
DI void mfma32az(f32x16& c, bf16x8 a, bf16x8 b) { asm volatile("v_mfma_f32_32x32x16_bf16 %0, %1, %2, 0" : "=&v"(c) : "v"(a), "a"(b)); }
DI void mfma32fa(f32x16& c, bf16x8 a, u32x4 b) { asm volatile("v_mfma_f32_32x32x16_bf16 %0, %1, %2, %0" : "+v"(c) : "a"(a), "v"(b)); }
DI void mfma32faz(f32x16& c, bf16x8 a, u32x4 b) { asm volatile("v_mfma_f32_32x32x16_bf16 %0, %1, %2, 0" : "=&v"(c) : "a"(a), "v"(b)); }
DI void mfma16z(f32x4& c, bf16x8 a, bf16x8 b) { asm volatile("v_mfma_f32_16x16x32_bf16 %0, %1, %2, 0" : "=&v"(c) : "v"(a), "v"(b)); }
#define NOP12(...) asm volatile("s_nop 7\n\ts_nop 3" : __VA_ARGS__)
DI u32x4 pk8(const f32x16& a, int o) { return (u32x4){pk2(a[o], a[o + 1]), pk2(a[o + 2], a[o + 3]), pk2(a[o + 4], a[o + 5]), pk2(a[o + 6], a[o + 7])}; }
DI u32x4 pk44(const f32x16& a, int oa, const f32x16& b, int ob) { return (u32x4){pk2(a[oa], a[oa + 1]), pk2(a[oa + 2], a[oa + 3]), pk2(b[ob], b[ob + 1]), pk2(b[ob + 2], b[ob + 3])}; }
constexpr int fidx(int T, int u) { return T == 0 ? (u == 4 ? 3 : u) : T == 1 ? 3 + u : 8 + u; }
constexpr bool isfirst(int kh, int m0) { return kh == 0 ? m0 < 5 : (m0 < 3 || m0 >= 8); }

DI void dual_softmax(const float (&p)[20], u32x4 (&o)[5]) {
  constexpr float L2E = 1.4426950408889634f;
  float mx = p[0], mn = p[0];
#pragma unroll
  for (int k = 1; k < 20; ++k) { mx = fmaxf(mx, p[k]); mn = fminf(mn, p[k]); }
  const float cx = -mx * L2E, cn = mn * L2E;
  float e[40], sp = 0.f, sn = 0.f;
#pragma unroll
  for (int k = 0; k < 20; ++k) {
    e[k] = __builtin_amdgcn_exp2f(__builtin_fmaf(p[k], L2E, cx));
    e[20 + k] = __builtin_amdgcn_exp2f(__builtin_fmaf(p[k], -L2E, cn));
    sp += e[k]; sn += e[20 + k];
  }
  const float rp = __builtin_amdgcn_rcpf(sp), rn = __builtin_amdgcn_rcpf(sn);
#pragma unroll
  for (int k = 0; k < 20; ++k) { e[k] *= rp; e[20 + k] *= rn; }
#pragma unroll
  for (int u = 0; u < 5; ++u) o[u] = (u32x4){pk2(e[8 * u], e[8 * u + 1]), pk2(e[8 * u + 2], e[8 * u + 3]), pk2(e[8 * u + 4], e[8 * u + 5]), pk2(e[8 * u + 6], e[8 * u + 7])};
}
DI void feature_mfma(const f32x16& a0, const f32x16& a1, const f32x16& a2, const bf16x8 (&wf)[13], f32x16 (&pp)[3]) {
  u32x4 kb[5] = {pk8(a0, 0), pk8(a0, 8), pk8(a1, 0), pk8(a1, 8), pk8(a2, 0)};
  asm volatile("s_nop 1" : "+v"(kb[0]), "+v"(kb[1]), "+v"(kb[2]), "+v"(kb[3]), "+v"(kb[4]));
  mfma32faz(pp[0], wf[fidx(0, 0)], kb[0]); mfma32faz(pp[1], wf[fidx(1, 1)], kb[1]); mfma32faz(pp[2], wf[fidx(2, 0)], kb[0]);
  mfma32fa(pp[0], wf[fidx(0, 1)], kb[1]); mfma32fa(pp[1], wf[fidx(1, 2)], kb[2]); mfma32fa(pp[2], wf[fidx(2, 1)], kb[1]);
  mfma32fa(pp[0], wf[fidx(0, 2)], kb[2]); mfma32fa(pp[1], wf[fidx(1, 3)], kb[3]); mfma32fa(pp[2], wf[fidx(2, 2)], kb[2]);
  mfma32fa(pp[2], wf[fidx(2, 3)], kb[3]);
  mfma32fa(pp[0], wf[fidx(0, 4)], kb[4]); mfma32fa(pp[2], wf[fidx(2, 4)], kb[4]); mfma32fa(pp[1], wf[fidx(1, 4)], kb[4]);
  NOP12("+v"(pp[0]), "+v"(pp[1]), "+v"(pp[2]));
}
template <int HD> DI void head_vals(const f32x16 (&pp)[3], float (&p)[20]) {
#pragma unroll
  for (int k = 0; k < 16; ++k) p[k] = pp[HD][k];
#pragma unroll
  for (int k = 0; k < 4; ++k) p[16 + k] = pp[2][4 * HD + k];
}

DI void chain_a(const Params& P, LAS char* lds, int c, int G, int rot, int sh, int w, int lane PROF_ARG) {
  CODE_PREFETCH(".Lca_beg", ".Lca_end");
  const int g = lane >> 4, i = lane & 15, h = lane >> 5, i32 = lane & 31, e3 = lane >> 3, pos = lane & 7, q = i >> 2, p = i & 3;
  const u32x4 sX = mksrd(P.Ax, 2048 * 1280), sW = mksrd(P.wqkv, 640 * 3840);
  const unsigned vx0 = e3 * 1280 + ((pos ^ (e3 >> 1)) << 4), vx1 = e3 * 1280 + ((pos ^ (4 + (e3 >> 1))) << 4);
  unsigned pk_[5], pu_[5], vW[5];
#pragma unroll
  for (int u = 0; u < 5; ++u) {
    const unsigned U = 64 * u + lane, pr = (U * 6554u) >> 16;
    pu_[u] = U - 10 * pr; pk_[u] = (pr & ~7u) | ((pr & 1u) << 2) | ((pr & 7u) >> 1);
    vW[u] = pk_[u] * 3840 + pu_[u] * 16;
  }
  const unsigned sx0 = (c * 128 + 32 * w) * 1280;
  const unsigned swq = (w & 1) * 32 * 3840 + G * 160, sw0 = swq + (1 + (w >> 1)) * 1280;
  const unsigned lbx = w * 4096, lbq = w * 5120, lbw = 16384 + lbq + (w >> 1) * 32;
  unsigned kc1 = rot * 128;
#define A1ISSUE(KC, U) do { constexpr int sl_ = ((KC) % 3) * RINGA; \
    if constexpr ((U) < 4) dma16<sl_ + (U) * 1024, (U) * 10240>(sX, ((U) & 1) ? vx1 : vx0, sx0 + kc1, lbx); \
    else dma16<sl_ + ((U) - 4) * 1024, 0>(sW, vW[(U) >= 4 ? (U) - 4 : 0], sw0 + kc1 * 1920, lbw); \
    if ((U) == 8) kadv(kc1); } while (0)
#define A1ALL(KC) do { A1ISSUE(KC, 0); A1ISSUE(KC, 4); A1ISSUE(KC, 5); A1ISSUE(KC, 1); A1ISSUE(KC, 6); A1ISSUE(KC, 2); A1ISSUE(KC, 7); A1ISSUE(KC, 3); A1ISSUE(KC, 8); } while (0)
  A1ALL(0);
  dma16<A_WPHI, 0>(mksrd(P.wphi, 32 * 800), lane * 16, G * 3200 + w * 1024, w * 1024);
  u64 tag = *(const CAS u64*)(P.ws + WS_EPOCH_A);
  if (w == 0) { *(LAS u32x4*)(lds + A_ZERO + lane * 16) = (u32x4){0u, 0u, 0u, 0u}; if (lane < 32) *(LAS u32x4*)(lds + A_ZERO + 1024 + lane * 16) = (u32x4){0u, 0u, 0u, 0u}; }
  if (w == 1 && lane == 0) *(LAS u32x4*)(lds + A_ONES) = (u32x4){0x3f80u, 0u, 0u, 0u};
  unsigned kq = rot * 128;
  if (w >= 2) kadv(kq);
#define AQISSUE(QC, U) do { dma16<qbase(QC) + (U) * 1024, 0>(sW, vW[U], swq + kq * 1920, lbq); if ((U) == 4) { kadv(kq); kadv(kq); } } while (0)
  LBase<4, 2> aX;
#pragma unroll
  for (int s = 0; s < 4; ++s) aX.v[0][s] = (w * 32 + i32) * 128 + (((2 * s + h) ^ ((i32 >> 1) & 7)) << 4);
  aX.fin();
  LBase<6, 2> aT;
  {
    const unsigned x = 2 * (g & 1) + (p >> 1), hh = p & 1, rowb = (8 * (g >> 1) + 2 * q) * 160;
    aT.v[0][0] = rowb + 8 * (x + 12 * hh);
    aT.v[0][1] = rowb + 8 * (4 + x + 12 * hh);
    aT.v[0][2] = rowb + (x < 2 ? 8 * (8 + x + 2 * hh) : 10272 + 8 * (x - 2 + 10 * hh));
    aT.v[0][3] = rowb + 10272 + 8 * (2 + x + 12 * hh);
    aT.v[0][4] = rowb + 10272 + 8 * (hh ? (x < 2 ? 12 + x : 16 + x) : 6 + x);
    aT.v[0][5] = rowb + 8 * (x < 2 ? 8 + x + 2 * hh : 2 + x + 10 * hh);
  }
  aT.fin();
  SCHED();
  A1ALL(1);
  STAMP(1);
  asm volatile("s_waitcnt vmcnt(9)" : "+v"(pf_dummy)::"memory"); bar();
  STAMP(2);
  asm volatile("" : "+s"(tag));
  tag += 0x9E3779B97F4A7C15ull;
  const unsigned tagl = (unsigned)tag, tagh = (unsigned)(tag >> 32);
  bf16x8 xf[40], fw[4][5];
  f32x16 acc[5];
#define A1RDW(KC, S, T) fw[S][T] = cat(ldstr(lds, aT.at<((KC) % 3) * RINGA + 16384 + (S) * 2560>(T)), ldstr(lds, aT.at<((KC) % 3) * RINGA + 16384 + (S) * 2560 + 160>(T)))
#define A1RDX(KC, S) xf[4 * (KC) + (S)] = ldsr128(lds, aX.at<((KC) % 3) * RINGA>(S))
#define A1RDG(KC, HF, N) do { constexpr int s0_ = 2 * (HF), s1_ = 2 * (HF) + 1; \
    if ((N) == 0) { A1RDX(KC, s0_); A1RDW(KC, s0_, 0); } if ((N) == 1) { A1RDW(KC, s0_, 1); A1RDW(KC, s0_, 2); } if ((N) == 2) { A1RDW(KC, s0_, 3); } \
    if ((N) == 3) { A1RDW(KC, s0_, 4); A1RDX(KC, s1_); } if ((N) == 4) { A1RDW(KC, s1_, 0); } if ((N) == 5) { A1RDW(KC, s1_, 1); A1RDW(KC, s1_, 2); } \
    if ((N) == 6) { A1RDW(KC, s1_, 3); } if ((N) == 7) { A1RDW(KC, s1_, 4); } } while (0)
  A1RDG(0, 0, 0); A1RDG(0, 0, 1); A1RDG(0, 0, 2); A1RDG(0, 0, 3); A1RDG(0, 0, 4); A1RDG(0, 0, 5); A1RDG(0, 0, 6); A1RDG(0, 0, 7);
  bf16x8 wf[13];
  {
    const bool same = (p & 1) == h;
    const unsigned xo = 2 * (g & 1) + (p >> 1);
    const unsigned b01 = A_WPHI + q * 40 + 8 * xo, b2 = A_WPHI + q * 40 + 32;
#define WFADDR(T, M0) ((same && ((T) == 2 ? (xo < 2 && ((xo == 0) == (h ? isfirst(1, M0) : isfirst(0, M0)))) : (h ? isfirst(1, M0) == ((T) == 0) : isfirst(0, M0) == ((T) == 0)))) \
                           ? ((T) == 2 ? b2 : b01) + (h ? ((M0) < 8 ? 1920 : 320) : 0) : (unsigned)A_ZERO)
#define WFBUILD(T, U) wf[fidx(T, U)] = cat(ldstr(lds, WFADDR(T, 2 * (U)) + 320 * (U)), ldstr(lds, WFADDR(T, 2 * (U) + 1) + 320 * (U) + 160))
    WFBUILD(0, 0); WFBUILD(0, 1); WFBUILD(0, 2); WFBUILD(0, 4); WFBUILD(1, 1); WFBUILD(1, 2); WFBUILD(1, 3); WFBUILD(1, 4);
    WFBUILD(2, 0); WFBUILD(2, 1); WFBUILD(2, 2); WFBUILD(2, 3); WFBUILD(2, 4);
#pragma unroll
    for (int k = 0; k < 13; ++k) asm volatile("" : "+a"(wf[k]));
  }
  SCHED();
#define A1GAP(KC, HF, X, RKC, RD, ISS) do { constexpr int s_ = 2 * (HF) + (X) / 5; \
    if ((KC) == 0 && (HF) == 0 && (X) < 5) mfma32az(acc[(X) % 5], fw[s_][(X) % 5], xf[4 * (KC) + s_]); \
    else mfma32a(acc[(X) % 5], fw[s_][(X) % 5], xf[4 * (KC) + s_]); \
    if constexpr (RD && (X) < 8) A1RDG(RKC, 1 - (HF), X); \
    if ((KC) == 0 && (HF) == 0) { if ((X) >= 1) A1ISSUE(2, ((X) >= 1 ? (X) - 1 : 0)); } \
    else { if ((X) == 2) { ISS(0); } if ((X) == 4) { ISS(1); } if ((X) == 6) { ISS(2); } if ((X) == 8) { ISS(3); } if ((X) == 9) { ISS(4); } } \
    SCHED(); } while (0)
#define A1HALF(KC, HF, RKC, RD, ISS) do { A1GAP(KC, HF, 0, RKC, RD, ISS); A1GAP(KC, HF, 1, RKC, RD, ISS); A1GAP(KC, HF, 2, RKC, RD, ISS); A1GAP(KC, HF, 3, RKC, RD, ISS); \
    A1GAP(KC, HF, 4, RKC, RD, ISS); A1GAP(KC, HF, 5, RKC, RD, ISS); A1GAP(KC, HF, 6, RKC, RD, ISS); A1GAP(KC, HF, 7, RKC, RD, ISS); A1GAP(KC, HF, 8, RKC, RD, ISS); A1GAP(KC, HF, 9, RKC, RD, ISS); } while (0)
#define A1STEP(KC, ISSA, VM, ISSB) do { A1HALF(KC, 0, KC, true, ISSA); if ((KC) < 9) vmwait<VM>(); bar(); A1HALF(KC, 1, ((KC) < 9 ? (KC) + 1 : 0), ((KC) < 9), ISSB); } while (0)
#define JA(KC, n) do { if constexpr ((n) < 4) A1ISSUE((KC) + 2, 5 + ((n) & 3)); } while (0)
#define JB(KC, n) A1ISSUE((KC) + 3, n)
#define JA1(n) JA(1, n)
#define JA2(n) JA(2, n)
#define JA3(n) JA(3, n)
#define JA4(n) JA(4, n)
#define JA5(n) JA(5, n)
#define JA6(n) JA(6, n)
#define JA7(n) JA(7, n)
#define JB0(n) JB(0, n)
#define JB1(n) JB(1, n)
#define JB2(n) JB(2, n)
#define JB3(n) JB(3, n)
#define JB4(n) JB(4, n)
#define JB5(n) JB(5, n)
#define JB6(n) JB(6, n)
#define JQ0(n) AQISSUE(0, n)
#define JQ1(n) AQISSUE(1, n)
#define JQ2(n) AQISSUE(2, n)
#define JQ3(n) AQISSUE(3, n)
  A1STEP(0, INONE, 9, JB0); A1STEP(1, JA1, 9, JB1); A1STEP(2, JA2, 9, JB2); A1STEP(3, JA3, 9, JB3); A1STEP(4, JA4, 9, JB4);
  A1STEP(5, JA5, 9, JB5); A1STEP(6, JA6, 9, JB6); A1STEP(7, JA7, 9, JQ0); A1STEP(8, INONE, 5, JQ1); A1STEP(9, JQ2, 0, JQ3);
  STAMP(3);
  NOP12("+v"(acc[0]), "+v"(acc[1]), "+v"(acc[2]), "+v"(acc[3]), "+v"(acc[4]));
  const unsigned rg = (w < 2 ? 0 : A_R1) + (32 * (w & 1) + i32) * 352, rv = (w < 2 ? 0 : A_R1) + 22528 + (32 * (w & 1) + i32) * 160;
  {
    f32x16 pp[3];
    feature_mfma(acc[0], acc[1], acc[2], wf, pp);
    *(LAS u32x4*)(lds + rv + 80 * h) = pk8(acc[2], 8);
    *(LAS u32x4*)(lds + rv + 16 + 96 * h) = pk8(acc[3], 0);
    *(LAS u32x4*)(lds + rv + 32 + 96 * h) = pk8(acc[3], 8);
    *(LAS u32x4*)(lds + rv + 48 + 48 * h) = pk8(acc[4], 0);
    *(LAS u32x4*)(lds + rv + 64 + 80 * h) = pk8(acc[4], 8);
    float pv[20];
    u32x4 ph[5];
    head_vals<0>(pp, pv); dual_softmax(pv, ph);
#pragma unroll
    for (int u = 0; u < 5; ++u) *(LAS u32x4*)(lds + rg + 160 * h + 16 * u) = ph[u];
    head_vals<1>(pp, pv); dual_softmax(pv, ph);
#pragma unroll
    for (int u = 0; u < 5; ++u) *(LAS u32x4*)(lds + rg + 160 * h + 80 + 16 * u) = ph[u];
  }
  bar();
  STAMP(4);
  const __amdgpu_buffer_rsrc_t rP = mkrs(P.ws + WS_KVP + (G * 4 + w) * 65536, 65536);
  {
    const unsigned tr = 16 * (g >> 1) + 4 * (g & 1) + q;
    LBase<1, 3> aP, aV;
    aP.v[0][0] = tr * 352 + 80 * w + 8 * p; aV.v[0][0] = 22528 + tr * 160 + 40 * w + 8 * p;
    aP.fin(); aV.fin();
    const unsigned v1b = p == 1 ? (unsigned)A_ONES : aV.v[0][0] + 32, v1m = p == 1 ? 0u : 1u;
    f32x4 kv[3][2];
#define KVSTEP(KS) do { constexpr int ro_ = ((KS) >> 1) * A_R1, r0_ = 32 * ((KS) & 1); \
      bf16x8 a_[3], b_[2]; \
      a_[0] = cat(ldstr(lds, aP.at<ro_ + r0_ * 352>(0)), ldstr(lds, aP.at<ro_ + (r0_ + 8) * 352>(0))); \
      b_[0] = cat(ldstr(lds, aV.at<ro_ + r0_ * 160>(0)), ldstr(lds, aV.at<ro_ + (r0_ + 8) * 160>(0))); \
      b_[1] = cat(ldstr(lds, v1b + v1m * (ro_ + r0_ * 160)), ldstr(lds, v1b + v1m * (ro_ + (r0_ + 8) * 160))); \
      a_[1] = cat(ldstr(lds, aP.at<ro_ + r0_ * 352 + 32>(0)), ldstr(lds, aP.at<ro_ + (r0_ + 8) * 352 + 32>(0))); \
      a_[2] = cat(ldstr(lds, aP.at<ro_ + r0_ * 352 + 64>(0)), ldstr(lds, aP.at<ro_ + (r0_ + 8) * 352 + 64>(0))); \
      _Pragma("unroll") for (int m = 0; m < 3; ++m) _Pragma("unroll") for (int n = 0; n < 2; ++n) { if ((KS) == 0) mfma16z(kv[m][n], a_[m], b_[n]); else mfma16(kv[m][n], a_[m], b_[n]); } } while (0)
    KVSTEP(0); KVSTEP(1); KVSTEP(2); KVSTEP(3);
    asm volatile("s_nop 7" : "+v"(kv[0][0]), "+v"(kv[0][1]), "+v"(kv[1][0]), "+v"(kv[1][1]), "+v"(kv[2][0]), "+v"(kv[2][1]));
#pragma unroll
    for (int m = 0; m < 3; ++m)
#pragma unroll
      for (int n = 0; n < 2; ++n) {
        const bool ok = (m < 2 || g < 2) && (n == 0 || i <= 4);
        __builtin_amdgcn_raw_buffer_store_b128(__builtin_bit_cast(u32x4, kv[m][n]), rP, ok ? ((4 * m + g) * 21 + 16 * n + i) * 16 : 0x7fffff00, c * 4096, 16);
      }
  }
  vmwait<6>(); bar();
  STAMP(5);
  AQISSUE(4, 0); AQISSUE(4, 1); AQISSUE(4, 2); AQISSUE(4, 3); AQISSUE(4, 4);
  f32x16 aq[3];
  bf16x8 fq[3][3];
  u32x4 hp[4];
#define AQOFF(K) (qbase((K) >> 3) + (((K) >> 2) & 1) * 10240 + ((K) & 3) * 2560)
#define AQRD(K, T) fq[(K) % 3][T] = cat(ldstr(lds, aT.at<AQOFF(K)>((T) == 2 ? 5 : (T))), ldstr(lds, aT.at<AQOFF(K) + 160>((T) == 2 ? 5 : (T))))
  AQRD(0, 0); AQRD(0, 1); AQRD(0, 2); AQRD(1, 0); AQRD(1, 1); AQRD(1, 2);
  SCHED();
#define AQGAP(K, T) do { if ((K) == 0) mfma32az(aq[T], fq[(K) % 3][T], xf[K]); else mfma32a(aq[T], fq[(K) % 3][T], xf[K]); \
    if constexpr ((K) + 2 < 40) AQRD((K) + 2, T); SCHED(); } while (0)
#define AQSTEP(K) do { AQGAP(K, 0); AQGAP(K, 1); AQGAP(K, 2); } while (0)
#define AQSTEP5(K) do { AQSTEP(K); AQSTEP(K + 1); AQSTEP(K + 2); AQSTEP(K + 3); AQSTEP(K + 4); } while (0)
  AQSTEP5(0); AQSTEP5(5); AQSTEP5(10);
  vmwait<5>();
  if (lane == 0) __hip_atomic_store((GAS u64*)(P.ws + WS_FA1 + ((G * 4 + w) * 16 + c) * 128), tag, __ATOMIC_RELAXED, __HIP_MEMORY_SCOPE_AGENT);
  SCHED();
  AQSTEP5(15); AQSTEP5(20); AQSTEP5(25);
  STAMP(6);
  poll_flags(P.ws + WS_FA1 + (G * 4 + w) * 2048, 128, 16, lane, tag);
  bar();
  STAMP(7);
  {
    const unsigned vo = g * 16384 + i * 16;
    const u32x4 sP = mksrd(P.ws + WS_KVP + (G * 4 + w) * 65536, 65536);
    const unsigned s0 = c * 256, s1 = s0 + 4096, s2 = s0 + 8192, s3 = s0 + 12288;
    asm volatile("s_nop 4\n\tbuffer_load_dwordx4 %0, %4, %5, %6 offen sc1\n\tbuffer_load_dwordx4 %1, %4, %5, %7 offen sc1\n\t"
                 "buffer_load_dwordx4 %2, %4, %5, %8 offen sc1\n\tbuffer_load_dwordx4 %3, %4, %5, %9 offen sc1"
                 : "=&v"(hp[0]), "=&v"(hp[1]), "=&v"(hp[2]), "=&v"(hp[3]) : "v"(vo), "s"(sP), "s"(s0), "s"(s1), "s"(s2), "s"(s3) : "memory");
  }
  SCHED();
  AQSTEP5(30); AQSTEP5(35);
  bar();
  STAMP(8);
  NOP12("+v"(aq[0]), "+v"(aq[1]), "+v"(aq[2]));
  f32x16 pq[3];
  feature_mfma(aq[0], aq[1], aq[2], wf, pq);
  const __amdgpu_buffer_rsrc_t rF = mkrs(P.ws + WS_KVF + (G * 4 + w) * 4096, 4096);
  {
    asm volatile("s_waitcnt vmcnt(0)" : "+v"(hp[0]), "+v"(hp[1]), "+v"(hp[2]), "+v"(hp[3])::"memory");
    float sv[4];
#pragma unroll
    for (int k = 0; k < 4; ++k) {
      float v = (__uint_as_float(hp[0][k]) + __uint_as_float(hp[1][k])) + (__uint_as_float(hp[2][k]) + __uint_as_float(hp[3][k]));
      const auto r = __builtin_amdgcn_permlane32_swap(__float_as_uint(v), __float_as_uint(v), false, false);
      const unsigned r0 = r[0], r1 = r[1];
      v = __uint_as_float(r0) + __uint_as_float(r1);
      const auto t = __builtin_amdgcn_permlane16_swap(__float_as_uint(v), __float_as_uint(v), false, false);
      const unsigned t0 = t[0], t1 = t[1];
      sv[k] = __uint_as_float(t0) + __uint_as_float(t1);
    }
    const u32x4 gr = {pk2(sv[0], sv[1]), tagl, pk2(sv[2], sv[3]), tagh};
    __builtin_amdgcn_raw_buffer_store_b128(gr, rF, g == 0 ? i * 16 : 0x7fffff00, c * 256, 16);
  }
  STAMP(9);
  const u32x4 sO = mksrd(P.wo, 640 * 1280);
  unsigned vO[5];
#pragma unroll
  for (int u = 0; u < 5; ++u) vO[u] = pk_[u] * 1280 + pu_[u] * 16;
  const unsigned so0 = ((w + sh) & 3) * 160 * 1280 + G * 160, lbo = w * 25600;
#define AOISSUE(N) dma16<(N) * 1024, ((N) / 5) * 40960>(sO, vO[(N) % 5], so0, lbo)
#define AOISSUE5(N) do { AOISSUE(N); AOISSUE(N + 1); AOISSUE(N + 2); AOISSUE(N + 3); AOISSUE(N + 4); } while (0)
  u32x4 phq[10];
  {
    float pv[20];
    u32x4 ph[5];
    AOISSUE5(0); AOISSUE5(5);
    head_vals<0>(pq, pv); dual_softmax(pv, ph);
#pragma unroll
    for (int u = 0; u < 5; ++u) { phq[u] = ph[u]; asm volatile("" : "+v"(phq[u])); }
    SCHED();
    AOISSUE5(10); AOISSUE5(15);
    head_vals<1>(pq, pv); dual_softmax(pv, ph);
#pragma unroll
    for (int u = 0; u < 5; ++u) { phq[5 + u] = ph[u]; asm volatile("" : "+v"(phq[5 + u])); }
    SCHED();
    AOISSUE5(20);
  }
  STAMP(10);
  {
    u32x4 gv[4];
    for (unsigned sp = 0; sp < SPIN_CAP; ++sp) {
      bool ok = true;
#pragma unroll
      for (int m = 0; m < 4; ++m) gv[m] = __builtin_amdgcn_raw_buffer_load_b128(rF, lane * 16, m * 1024, 16);
#pragma unroll
      for (int m = 0; m < 4; ++m) ok &= (lane + 64 * m >= 210) | ((gv[m][1] == tagl) & (gv[m][3] == tagh));
      if (__all(ok)) break;
      asm volatile("" ::: "memory");
    }
#pragma unroll
    for (int m = 0; m < 4; ++m) {
      const unsigned sl = lane + 64 * m, f4 = (sl * 3121u) >> 16, d = sl - 21 * f4;
      if (sl < 210) *(LAS u32x2*)(lds + A_KVT + w * 1680 + d * 80 + f4 * 8) = (u32x2){gv[m][0], gv[m][2]};
    }
  }
  bar();
  STAMP(11);
  const __amdgpu_buffer_rsrc_t rOb = mkrs(P.ws + WS_OB + c * 163840, 163840);
  const unsigned vol = w * 5120 + lane * 16;
  {
    const bool val = ((i32 >> 2) & 1) == h;
    const unsigned xo = i32 >> 3, e = i32 & 3, hb = A_KVT + 2 * h * 1680;
    const unsigned a0 = val ? hb + (e + 4 * xo) * 80 : (unsigned)A_ZERO, a1 = val ? a0 + 1680 : (unsigned)A_ZERO;
    const unsigned a2 = (val && !(xo & 1)) ? hb + (xo == 0 ? 16 + e : 20) * 80 : (unsigned)A_ZERO;
    const unsigned a3 = (val && (xo & 1)) ? hb + 1680 + (xo == 1 ? 16 + e : 20) * 80 : (unsigned)A_ZERO;
    bf16x8 fa_[4][5];
#pragma unroll
    for (int u = 0; u < 5; ++u) { fa_[0][u] = ldsr128(lds, a0 + 16 * u); fa_[1][u] = ldsr128(lds, a1 + 16 * u); fa_[2][u] = ldsr128(lds, a2 + 16 * u); fa_[3][u] = ldsr128(lds, a3 + 16 * u); }
    f32x16 oo[3];
#pragma unroll
    for (int u = 0; u < 5; ++u) {
      if (u == 0) { mfma32z(oo[0], fa_[0][u], __builtin_bit_cast(bf16x8, phq[u])); mfma32z(oo[1], fa_[1][u], __builtin_bit_cast(bf16x8, phq[5 + u])); mfma32z(oo[2], fa_[2][u], __builtin_bit_cast(bf16x8, phq[u])); }
      else { mfma32(oo[0], fa_[0][u], __builtin_bit_cast(bf16x8, phq[u])); mfma32(oo[1], fa_[1][u], __builtin_bit_cast(bf16x8, phq[5 + u])); mfma32(oo[2], fa_[2][u], __builtin_bit_cast(bf16x8, phq[u])); }
      mfma32(oo[2], fa_[3][u], __builtin_bit_cast(bf16x8, phq[5 + u]));
    }
    NOP12("+v"(oo[0]), "+v"(oo[1]), "+v"(oo[2]));
    const float r0 = __builtin_amdgcn_rcpf(oo[2][8] + 1e-6f), r1 = __builtin_amdgcn_rcpf(oo[2][12] + 1e-6f);
#pragma unroll
    for (int k = 0; k < 16; ++k) { oo[0][k] *= r0; oo[1][k] *= r1; }
#pragma unroll
    for (int k = 0; k < 4; ++k) { oo[2][k] *= r0; oo[2][4 + k] *= r1; }
    const u32x4 of[5] = {pk8(oo[0], 0), pk8(oo[0], 8), pk44(oo[2], 0, oo[1], 0), pk8(oo[1], 4), pk44(oo[1], 12, oo[2], 4)};
#pragma unroll
    for (int u = 0; u < 5; ++u) __builtin_amdgcn_raw_buffer_store_b128(of[u], rOb, vol, G * 20480 + u * 1024, 16);
  }
  STAMP(12);
  vmwait<0>(); bar();
  if (w == 0 && lane == 0) __hip_atomic_store((GAS u64*)(P.ws + WS_FA2 + (c * 8 + G) * 128), tag, __ATOMIC_RELAXED, __HIP_MEMORY_SCOPE_AGENT);
  STAMP(13);
  LBase<3, 2> aO;
  aO.v[0][0] = (40 * (g >> 1) + 2 * q) * 160 + 32 * (g & 1) + 8 * p;
  aO.v[0][1] = (40 * (g >> 1) + 8 * (g & 1) + 2 * q) * 160 + 128 + 8 * p;
  aO.v[0][2] = (40 * (g >> 1) + 48 * (g & 1) + 2 * q) * 160 + 128 + 8 * p;
  aO.fin();
  poll_flags(P.ws + WS_FA2 + c * 1024, 128, 8, lane, tag);
  STAMP(14);
  if (c == 0 && G == 0 && w == 0 && lane == 0) __hip_atomic_store((GAS u64*)(P.ws + WS_EPOCH_A), tag, __ATOMIC_RELAXED, __HIP_MEMORY_SCOPE_AGENT);
  u32x4 ofr[40];
#pragma unroll
  for (int S = 0; S < 40; ++S) ofr[S] = __builtin_amdgcn_raw_buffer_load_b128(rOb, vol, ((S / 5 + 2 * sh) & 7) * 20480 + (S % 5) * 1024, 16);
  f32x16 y[2];
  f32x4 z[2] = {(f32x4){0.f, 0.f, 0.f, 0.f}, (f32x4){0.f, 0.f, 0.f, 0.f}};
  bf16x8 fo[2][5];
#define AOOFF(S) ((80 * ((S) / 5) + 8 * ((S) % 5)) * 160)
#define A2RD(M, N) fo[(M) & 1][N] = (N) < 4 ? cat(ldstr(lds, aO.at<AOOFF(2 * (M) + ((N) >> 1 & 1)) + ((N) & 1) * 64>(0)), ldstr(lds, aO.at<AOOFF(2 * (M) + ((N) >> 1 & 1)) + ((N) & 1) * 64 + 160>(0))) \
                                            : cat(ldstr(lds, aO.at<AOOFF(2 * (M))>((2 * (M)) % 5 == 4 ? 2 : 1)), ldstr(lds, aO.at<AOOFF(2 * (M)) + 160>((2 * (M)) % 5 == 4 ? 2 : 1)))
#define A2RDX(M, N) do { if constexpr ((M) < 20) A2RD(M, N); } while (0)
  A2RD(0, 0); A2RD(0, 1); A2RD(0, 2); A2RD(0, 3); A2RD(0, 4);
  SCHED();
#define A2STEP(M) do { u32x4 f0 = ofr[2 * (M)], f1 = ofr[2 * (M) + 1]; \
    if ((M) == 0) mfma32z(y[0], __builtin_bit_cast(bf16x8, f0), fo[(M) & 1][0]); else mfma32(y[0], __builtin_bit_cast(bf16x8, f0), fo[(M) & 1][0]); \
    A2RDX((M) + 1, 0); SCHED(); \
    if ((M) == 0) mfma32z(y[1], __builtin_bit_cast(bf16x8, f0), fo[(M) & 1][1]); else mfma32(y[1], __builtin_bit_cast(bf16x8, f0), fo[(M) & 1][1]); \
    A2RDX((M) + 1, 1); A2RDX((M) + 1, 2); SCHED(); \
    mfma32(y[0], __builtin_bit_cast(bf16x8, f1), fo[(M) & 1][2]); \
    A2RDX((M) + 1, 3); A2RDX((M) + 1, 4); SCHED(); \
    mfma32(y[1], __builtin_bit_cast(bf16x8, f1), fo[(M) & 1][3]); \
    swap16x4(f0, f1); SCHED(); \
    mfma16x2(z[0], z[1], f0, f1, fo[(M) & 1][4]); SCHED(); } while (0)
#define A2STEP4(M) do { A2STEP(M); A2STEP(M + 1); A2STEP(M + 2); A2STEP(M + 3); } while (0)
  A2STEP4(0); A2STEP4(4); A2STEP4(8); A2STEP4(12); A2STEP4(16);
  STAMP(15);
  NOP12("+v"(y[0]), "+v"(y[1]), "+v"(z[0]), "+v"(z[1]));
  {
    const __amdgpu_buffer_rsrc_t rO = mkrs(P.out, 2048 * 5120);
    const unsigned so = (c * 128 + 32 * w) * 5120 + G * 320;
#pragma unroll
    for (int r = 0; r < 16; ++r) {
      const unsigned v = pk2(y[0][r], y[1][r]);
      __builtin_amdgcn_raw_buffer_store_b32(v << 16, rO, h * 20480 + i32 * 4, so + ((r & 3) + 8 * (r >> 2)) * 5120, 16);
      __builtin_amdgcn_raw_buffer_store_b32(v & 0xffff0000u, rO, h * 20480 + 128 + i32 * 4, so + ((r & 3) + 8 * (r >> 2)) * 5120, 16);
    }
#pragma unroll
    for (int r = 0; r < 4; ++r) {
      const unsigned v = pk2(z[0][r], z[1][r]);
      __builtin_amdgcn_raw_buffer_store_b32(v << 16, rO, g * 20480 + 256 + i * 4, so + r * 5120, 16);
      __builtin_amdgcn_raw_buffer_store_b32(v & 0xffff0000u, rO, g * 20480 + 256 + i * 4, so + (16 + r) * 5120, 16);
    }
  }
  CODE_END(".Lca_end");
}

__global__ __launch_bounds__(256, 1) void fused_kernel(Params P) {
  __shared__ __attribute__((aligned(16))) char smem[163840];
  LAS char* lds = (LAS char*)smem;
  const int b = blockIdx.x, xcd = b & 7, loc = b >> 3, l = loc & 15;
  const int c = (xcd >> 1) * 4 + (l & 3), j = (xcd & 1) * 4 + (l >> 2);
  const int rot = ((((l & 3) + (l >> 2)) & 3) * 5) >> 1;
  const int w = __builtin_amdgcn_readfirstlane(threadIdx.x >> 6), lane = threadIdx.x & 63;
  STAMP(0);
  if (loc >= 16) chain_b(P, lds, c, j, rot, w, lane PROF_PASS);
  else chain_a(P, lds, c, j, rot, l >> 2, w, lane PROF_PASS);
  PROF_FLUSH();
}

extern "C" void kernel_launch(void* const* d_in, const int* in_sizes, int n_in, void* d_out, int out_size, void* d_ws,
                              size_t ws_size, hipStream_t stream) {
  Params P;
  P.Ax = (const u16*)d_in[0]; P.wqkv = (const u16*)d_in[1]; P.wphi = (const u16*)d_in[2]; P.wo = (const u16*)d_in[3];
  P.Bx = (const u16*)d_in[4]; P.lre = (const u16*)d_in[5]; P.lim = (const u16*)d_in[6]; P.bre = (const u16*)d_in[7];
  P.bim = (const u16*)d_in[8]; P.cre = (const u16*)d_in[9]; P.cim = (const u16*)d_in[10]; P.logdt = (const u16*)d_in[11];
  P.dsk = (const u16*)d_in[12]; P.out = (float*)d_out; P.ws = (char*)d_ws;
  fused_kernel<<<256, 256, 0, stream>>>(P);
}
```

```cpp
#define PROF
#define WBL
#define DIAG_K 0
#define DIAG_K0 0
#define DIAG_B 0
#include <hip/hip_runtime.h>
#include <hip/hip_bf16.h>
#include <stdint.h>

typedef unsigned short u16;
typedef unsigned long long u64;
typedef __bf16 bf16x8 __attribute__((ext_vector_type(8)));
typedef __bf16 bf16x4 __attribute__((ext_vector_type(4)));
typedef __bf16 bf16x2 __attribute__((ext_vector_type(2)));
typedef float f32x4 __attribute__((ext_vector_type(4)));
typedef float f32x2 __attribute__((ext_vector_type(2)));
typedef unsigned u32x4 __attribute__((ext_vector_type(4)));
typedef unsigned u32x2 __attribute__((ext_vector_type(2)));
#define LAS __attribute__((address_space(3)))
#define GAS __attribute__((address_space(1)))
#define CAS __attribute__((address_space(4)))
#define DI __device__ __forceinline__
#define SCHED() __builtin_amdgcn_sched_barrier(0)
typedef float f32x16 __attribute__((ext_vector_type(16)));

constexpr unsigned WS_EPOCH_A = 0, WS_EPOCH_B = 256;
constexpr unsigned WS_FA1 = 4096;
constexpr unsigned WS_FA2 = WS_FA1 + 512 * 128;
constexpr unsigned WS_FB2 = WS_FA2 + 128 * 128;
constexpr unsigned WS_GB1 = WS_FB2 + 128 * 128;
constexpr unsigned WS_KVF = WS_GB1 + 128 * 1280;
constexpr unsigned WS_KVP = WS_KVF + 32 * 4096;
constexpr unsigned WS_OB = WS_KVP + 512 * 4096;
constexpr unsigned WS_HB = WS_OB + 1024 * 2560;
constexpr unsigned WS_END = WS_HB + 16 * 320 * 1024;
#define PROF_OFF (12u << 20)
#define PROF_N 20
constexpr unsigned SPIN_CAP = 1u << 17;

struct Params {
  const u16 *Ax, *wqkv, *wphi, *wo, *Bx, *lre, *lim, *bre, *bim, *cre, *cim, *logdt, *dsk;
  float* out;
  char* ws;
};

DI float bf2f(unsigned v) { return __uint_as_float(v << 16); }
DI float rbf(float f) { return (float)(__bf16)f; }
DI unsigned pk2(float a, float b) { f32x2 f = {a, b}; return __builtin_bit_cast(unsigned, __builtin_convertvector(f, bf16x2)); }
DI u32x4 mksrd(const void* p, unsigned bytes) {
  const u64 a = (u64)p;
  u32x4 r = {(unsigned)a, (unsigned)(a >> 32) & 0xffffu, bytes, 0x00020000u};
  return r;
}
DI __amdgpu_buffer_rsrc_t mkrs(const void* p, unsigned bytes) { return __builtin_amdgcn_make_buffer_rsrc((void*)p, 0, bytes, 0x00020000); }
template <int LI, int SI>
DI void dma16(u32x4 srd, unsigned vo, unsigned so, unsigned lb) {
  unsigned t;
  asm volatile("s_add_u32 m0, %[lb], %[li]\n\ts_add_u32 %[t], %[so], %[si]\n\tbuffer_load_dwordx4 %[vo], %[srd], %[t] offen lds"
               : [t] "=&s"(t) : [lb] "s"(lb), [li] "i"(LI), [so] "s"(so), [si] "i"(SI), [vo] "v"(vo), [srd] "s"(srd) : "memory", "scc");
}
DI void kadv(unsigned& k) { asm volatile("s_add_u32 %0, %0, 0x80\n\ts_cmp_eq_u32 %0, 0x500\n\ts_cselect_b32 %0, 0, %0" : "+s"(k)::"scc"); }
DI void mfma32(f32x16& c, bf16x8 a, bf16x8 b) { asm volatile("v_mfma_f32_32x32x16_bf16 %0, %1, %2, %0" : "+v"(c) : "v"(a), "v"(b)); }
DI void mfma32z(f32x16& c, bf16x8 a, bf16x8 b) { asm volatile("v_mfma_f32_32x32x16_bf16 %0, %1, %2, 0" : "=&v"(c) : "v"(a), "v"(b)); }
DI void mfma16(f32x4& c, bf16x8 a, bf16x8 b) { asm volatile("v_mfma_f32_16x16x32_bf16 %0, %1, %2, %0" : "+v"(c) : "v"(a), "v"(b)); }
DI void swap16x4(u32x4& a, u32x4& b) {
#pragma unroll
  for (int k = 0; k < 4; ++k) {
    const auto r = __builtin_amdgcn_permlane16_swap(a[k], b[k], false, false);
    const unsigned r0 = r[0], r1 = r[1];
    a[k] = r0; b[k] = r1;
  }
}
DI void mfma16x2(f32x4& c0, f32x4& c1, u32x4 a0, u32x4 a1, bf16x8 b) {
  asm volatile("s_nop 1\n\tv_mfma_f32_16x16x32_bf16 %0, %2, %4, %0\n\tv_mfma_f32_16x16x32_bf16 %1, %3, %4, %1" : "+v"(c0), "+v"(c1) : "v"(a0), "v"(a1), "v"(b));
}
template <int N, int NW> struct LBase {
  unsigned v[NW][N];
  DI void fin() {
#pragma unroll
    for (int k = 1; k < NW; ++k)
#pragma unroll
      for (int n = 0; n < N; ++n) { v[k][n] = v[0][n] + k * 65536; asm volatile("" : "+v"(v[k][n])); }
  }
  template <int OFF> DI unsigned at(int n) const { return v[OFF >> 16][n] + (OFF & 65535); }
};
template <int N> DI void vmwait() { asm volatile("s_waitcnt vmcnt(%0)" ::"i"(N) : "memory"); }
DI void bar() { asm volatile("s_waitcnt lgkmcnt(0)\n\ts_barrier" ::: "memory"); }
DI float dppx(float x) { return __builtin_bit_cast(float, __builtin_amdgcn_update_dpp(0, __builtin_bit_cast(int, x), 0xB1, 0xF, 0xF, true)); }
DI bf16x8 ldsr128(LAS char* l, unsigned off) { return *(LAS bf16x8*)(l + off); }
DI bf16x4 ldstr(LAS char* l, unsigned off) { return __builtin_amdgcn_ds_read_tr16_b64_v4bf16((LAS bf16x4*)(l + off)); }
DI bf16x8 cat(bf16x4 a, bf16x4 b) { return __builtin_shufflevector(a, b, 0, 1, 2, 3, 4, 5, 6, 7); }
struct Prof { unsigned r[PROF_N], c[PROF_N]; };
#define STAMP(k) do { SCHED(); u64 _t, _c; asm volatile("s_memrealtime %0\n\ts_memtime %1\n\ts_waitcnt lgkmcnt(0)" : "=&s"(_t), "=&s"(_c)::"memory"); \
    pf.r[k] = (unsigned)_t; pf.c[k] = (unsigned)_c; SCHED(); } while (0)
#define PROF_ARG , Prof& pf
#define PROF_PASS , pf
#define PROF_FLUSH() do { if (lane == 0) { _Pragma("unroll") for (int k_ = 0; k_ < PROF_N; ++k_) { ((u64*)(P.ws + PROF_OFF))[(blockIdx.x * 4 + w) * PROF_N + k_] = pf.r[k_]; \
    ((u64*)(P.ws + PROF_OFF + (1u << 20)))[(blockIdx.x * 4 + w) * PROF_N + k_] = pf.c[k_]; } } } while (0)

DI void scan_step(float& o0, float& o1, float& o2, float& o3, float& o4, float u0, float u1, float u2, float u3, float u4,
                  const float (&s)[5], const float (&a)[5], const float (&b)[5]) {
  asm("v_fma_f32 %0, %15, %10, %5\n\tv_fma_f32 %1, %16, %11, %6\n\tv_fma_f32 %2, %17, %12, %7\n\tv_fma_f32 %3, %18, %13, %8\n\tv_fma_f32 %4, %19, %14, %9\n\t"
      "v_fmac_f32_dpp %0, %10, %20 quad_perm:[1,0,3,2] row_mask:0xf bank_mask:0xf\n\t"
      "v_fmac_f32_dpp %1, %11, %21 quad_perm:[1,0,3,2] row_mask:0xf bank_mask:0xf\n\t"
      "v_fmac_f32_dpp %2, %12, %22 quad_perm:[1,0,3,2] row_mask:0xf bank_mask:0xf\n\t"
      "v_fmac_f32_dpp %3, %13, %23 quad_perm:[1,0,3,2] row_mask:0xf bank_mask:0xf\n\t"
      "v_fmac_f32_dpp %4, %14, %24 quad_perm:[1,0,3,2] row_mask:0xf bank_mask:0xf"
      : "=&v"(o0), "=&v"(o1), "=&v"(o2), "=&v"(o3), "=&v"(o4)
      : "v"(u0), "v"(u1), "v"(u2), "v"(u3), "v"(u4), "v"(s[0]), "v"(s[1]), "v"(s[2]), "v"(s[3]), "v"(s[4]),
        "v"(a[0]), "v"(a[1]), "v"(a[2]), "v"(a[3]), "v"(a[4]), "v"(b[0]), "v"(b[1]), "v"(b[2]), "v"(b[3]), "v"(b[4]));
}

DI void poll_flags(const char* base, unsigned stride, int n, int lane, u64 tag) {
  const GAS u64* fp = (const GAS u64*)(base + (unsigned)(lane < n ? lane : 0) * stride);
  for (unsigned sp = 0; sp < SPIN_CAP; ++sp) {
    const u64 v = __hip_atomic_load(fp, __ATOMIC_RELAXED, __HIP_MEMORY_SCOPE_AGENT);
    if (__all(v == tag)) break;
    __builtin_amdgcn_s_sleep(1);
  }
  asm volatile("" ::: "memory");
}

constexpr int RING = 36864, TB = 110592, T_PAR = TB, T_POW = TB + 2560, T_E = TB + 7680, T_L = TB + 12800, T_H = 161792;

template <int KC, int U>
DI void b1_dma(u32x4 sX, u32x4 sW, unsigned vx0, unsigned vx1, unsigned vw, unsigned sox, unsigned sow, unsigned lbx, unsigned lbw) {
  constexpr int slot = (KC % 3) * RING;
  if constexpr (U < 4) dma16<slot + U * 1024, U * 4 * 1280>(sX, (U & 1) ? vx1 : vx0, sox, lbx);
  else dma16<slot + 16384 + (U - 4) * 1024, (U - 4) * 10240>(sW, vw, sow, lbw);
}
constexpr int cbase(int ci, int part) {
  return ci == 3 ? part * 10240 : ci == 4 ? (part ? 98304 : 20480) : (ci == 5 || ci == 6) ? TB + (ci - 5) * 20480 + part * 10240
                                : 36864 + (ci >= 7 ? ci - 7 : ci) * 20480 + part * 10240;
}

constexpr int late_dma(int ks, int slot) {
  int n = 0;
  for (int k = 11; k <= 21; ++k)
    for (int sl = 0; sl < 2; ++sl) {
      const bool on = sl == 0 || k == 12 || k == 14 || k == 16 || k == 18;
      if (k == ks && sl == slot) return on ? n : -1;
      if (on) ++n;
    }
  return -1;
}
struct Disc { float ld, a, b, dt, e, y, n, r, z, sp, cp, lr, li, cr, ci, t0, t1, t2, qr, qi, pr, pi; };
#define PIN1(x) asm volatile("" : "+v"(x))
#define PIN2(x, y) asm volatile("" : "+v"(x), "+v"(y))
#define PIN3(x, y, z) asm volatile("" : "+v"(x), "+v"(y), "+v"(z))
template <int K>
DI void disc_slice(Disc& D, LAS char* lds, int pl) {
  constexpr float L2E = 1.4426950408889634f;
  if constexpr (K == 0) { D.dt = rbf(__builtin_amdgcn_exp2f(D.ld * L2E)); PIN1(D.dt); }
  if constexpr (K == 1) { D.e = __builtin_amdgcn_exp2f((D.dt * D.a) * L2E); D.y = D.dt * D.b; PIN2(D.e, D.y); }
  if constexpr (K == 2) { D.n = __builtin_rintf(D.y * 0.6366197723675814f); D.r = __builtin_fmaf(D.n, -1.5703125f, D.y); PIN2(D.n, D.r); }
  if constexpr (K == 3) { D.r = __builtin_fmaf(D.n, -4.837512969970703125e-4f, D.r); D.r = __builtin_fmaf(D.n, -7.54978995489188216e-8f, D.r); D.z = D.r * D.r; PIN2(D.r, D.z); }
  if constexpr (K == 4) { D.sp = __builtin_fmaf(__builtin_fmaf(D.z, -1.9515295891e-4f, 8.3321608736e-3f), D.z, -1.6666654611e-1f); D.t0 = D.r * D.z; PIN2(D.sp, D.t0); }
  if constexpr (K == 5) { D.sp = __builtin_fmaf(D.sp, D.t0, D.r); D.cp = __builtin_fmaf(__builtin_fmaf(D.z, 2.443315711809948e-5f, -1.388731625493765e-3f), D.z, 4.166664568298827e-2f); PIN2(D.sp, D.cp); }
  if constexpr (K == 6) { D.t0 = D.z * D.z; D.t1 = __builtin_fmaf(D.z, -0.5f, 1.f); D.cp = __builtin_fmaf(D.cp, D.t0, D.t1); PIN1(D.cp); }
  if constexpr (K == 7) { const int q = (int)D.n; D.t2 = __int_as_float(q); D.t0 = (q & 1) ? D.cp : D.sp; D.t1 = (q & 1) ? D.sp : D.cp; PIN3(D.t0, D.t1, D.t2); }
  if constexpr (K == 8) { const unsigned q = __float_as_uint(D.t2); D.t0 = __uint_as_float(__float_as_uint(D.t0) ^ ((q & 2u) << 30)); D.t2 = __uint_as_float(((q + 1u) & 2u) << 30); PIN2(D.t0, D.t2); }
  if constexpr (K == 9) { D.t1 = __uint_as_float(__float_as_uint(D.t1) ^ __float_as_uint(D.t2)); D.li = D.e * D.t0; D.lr = D.e * D.t1; PIN2(D.lr, D.li); }
  if constexpr (K == 10) { D.t0 = __builtin_amdgcn_rcpf(fmaxf(fabsf(D.a), fabsf(D.b))); D.t2 = D.lr - 1.f; PIN2(D.t0, D.t2); }
  if constexpr (K == 11) { D.a *= D.t0; D.b *= D.t0; D.t1 = D.a * D.a; PIN3(D.a, D.b, D.t1); }
  if constexpr (K == 12) { D.t1 = __builtin_amdgcn_rcpf(__builtin_fmaf(D.b, D.b, D.t1)); D.cr = D.t2 * D.a; PIN2(D.t1, D.cr); }
  if constexpr (K == 13) { D.t1 *= D.t0; D.cr = __builtin_fmaf(D.li, D.b, D.cr); D.ci = D.li * D.a; PIN3(D.t1, D.cr, D.ci); }
  if constexpr (K == 14) { D.ci = __builtin_fmaf(-D.t2, D.b, D.ci) * D.t1; D.cr *= D.t1; PIN2(D.cr, D.ci); }
  if constexpr (K == 15) { *(LAS f32x4*)(lds + T_PAR + pl * 32) = (f32x4){D.lr, D.li, D.cr, D.ci}; D.qr = D.lr; D.qi = D.li; }
  if constexpr (K >= 16 && K < 20) { const float t = __builtin_fmaf(D.qr, D.qr, -D.qi * D.qi); D.qi = (D.qr + D.qr) * D.qi; D.qr = t; PIN2(D.qr, D.qi); }
  if constexpr (K == 20) { *(LAS f32x2*)(lds + T_POW + pl * 8) = (f32x2){1.f, 0.f}; D.pr = D.qr; D.pi = D.qi; }
  if constexpr (K >= 21 && K < 28) {
    *(LAS f32x2*)(lds + T_POW + ((K - 20) * 80 + pl) * 8) = (f32x2){D.pr, D.pi};
    const float t = __builtin_fmaf(D.pr, D.qr, -D.pi * D.qi); D.pi = __builtin_fmaf(D.pr, D.qi, D.pi * D.qr); D.pr = t; PIN2(D.pr, D.pi);
  }
  if constexpr (K == 28) *(LAS f32x4*)(lds + T_PAR + pl * 32 + 16) = (f32x4){D.pr, D.pi, D.qr, D.qi};
}

#define CODE_PREFETCH(BEG, END) \
  unsigned pf_dummy; \
  { u64 pc_; unsigned sz_; \
    asm volatile("s_getpc_b64 %0\n" BEG ":\n\ts_mov_b32 %1, " END "-" BEG : "=s"(pc_), "=s"(sz_)); \
    const u32x4 sp_ = mksrd((const void*)pc_, sz_); const unsigned vo_ = (w * 64 + lane) * 128; \
    asm volatile("buffer_load_dword %0, %1, %2, 0 offen" : "=&v"(pf_dummy) : "v"(vo_), "s"(sp_) : "memory"); }
#define CODE_END(END) asm volatile(END ":" ::: "memory")
DI void chain_b(const Params& P, LAS char* lds, int c, int j, int rot, int w, int lane PROF_ARG) {
  CODE_PREFETCH(".Lcb_beg", ".Lcb_end");
  const int g = lane >> 4, i = lane & 15, h = lane >> 5, i32 = lane & 31, e3 = lane >> 3, pos = lane & 7, wr = w >> 1, wc = w & 1;
  const u32x4 sX = mksrd(P.Bx, 2048 * 1280), sW = mksrd(wr ? P.bim : P.bre, 640 * 1280);
  const unsigned vxr = (16 * (e3 >> 2) + (e3 & 3)) * 1280;
  const unsigned vx0 = vxr + ((pos ^ (e3 >> 1)) << 4), vx1 = vxr + ((pos ^ (4 + (e3 >> 1))) << 4);
  const unsigned vw = e3 * 1280 + ((pos ^ ((e3 & 6) | wr)) << 4);
  const unsigned sx0 = (c * 128 + 32 * w) * 1280, sw0 = (j * 80 + 40 * wc) * 1280;
  const unsigned lbx = w * 4096, lbw = w * 5120;
  unsigned kc1 = rot * 128;
#define B1ISSUE(KC, U) do { b1_dma<KC, U>(sX, sW, vx0, vx1, vw, sx0 + kc1, sw0 + kc1, lbx, lbw); if ((U) == 8) kadv(kc1); } while (0)
#define B1ALL(KC) do { B1ISSUE(KC, 0); B1ISSUE(KC, 4); B1ISSUE(KC, 5); B1ISSUE(KC, 1); B1ISSUE(KC, 6); B1ISSUE(KC, 2); B1ISSUE(KC, 7); B1ISSUE(KC, 3); B1ISSUE(KC, 8); } while (0)
  const int plc = 20 * w + (lane < 20 ? lane : 0);
  unsigned in0, in1, in2;
  { u32x4 t_ = sX; asm volatile("" : "+s"(t_)); STAMP(1); }
  B1ISSUE(0, 0);
  STAMP(2);
  B1ISSUE(0, 4); B1ISSUE(0, 5); B1ISSUE(0, 1); B1ISSUE(0, 6);
  STAMP(3);
  B1ISSUE(0, 2); B1ISSUE(0, 7); B1ISSUE(0, 3); B1ISSUE(0, 8);
  STAMP(4);
  {
    const unsigned po = (j * 80 + plc) * 2;
    asm volatile("global_load_ushort %0, %3, %4\n\tglobal_load_ushort %1, %3, %5\n\tglobal_load_ushort %2, %3, %6"
                 : "=&v"(in0), "=&v"(in1), "=&v"(in2) : "v"(po), "s"(P.logdt), "s"(P.lre), "s"(P.lim) : "memory");
  }
  u64 tag = *(const CAS u64*)(P.ws + WS_EPOCH_B);
  const u32x4 sC = mksrd(wr ? P.cim : P.cre, 640 * 1280);
  unsigned vC[5];
#pragma unroll
  for (int u = 0; u < 5; ++u) {
    const int R = 8 * (5 * wc + u) + e3, d = R < 64 ? 2 * (R & 31) + (R >> 5) : R;
    vC[u] = (j * 80 + d) * 1280 + ((pos ^ ((R >> 1) & 7)) << 4);
  }
  const unsigned lb4 = (wr ? 98304 : 20480) + wc * 5120;
  unsigned kc2 = rot * 128;
#define B2ISSUE(CI, U) do { dma16<((CI) == 4 ? 0 : cbase(CI, 0)) + (U) * 1024, 0>(sC, vC[U], kc2, (CI) == 4 ? lb4 : lbw); if ((U) == 4) kadv(kc2); } while (0)
  f32x16 acc[5];
  const unsigned ra = (w * 32 + i32) * 128, rb = 16384 + ((i32 & 1) * 80 + (i32 >> 1)) * 128;
  const unsigned ma = (i32 >> 1) & 7, mb = (((i32 >> 2) & 3) << 1) | (i32 & 1);
  LBase<4, 2> aA, aB;
#pragma unroll
  for (int s = 0; s < 4; ++s) { aA.v[0][s] = ra + (((2 * s + h) ^ ma) << 4); aB.v[0][s] = rb + (((2 * s + h) ^ mb) << 4); }
  aA.fin(); aB.fin();
  bf16x8 fa[2][2], fb[2][2][5];
#define B1RD(KC, HF, Q) do { constexpr int sl = ((KC) % 3) * RING, s2 = (Q) / 6, q6 = (Q) % 6; \
    if (q6 == 0) fa[HF][s2] = ldsr128(lds, aA.at<sl>(2 * (HF) + s2)); else fb[HF][s2][q6 - 1] = ldsr128(lds, aB.at<sl + (q6 > 0 ? q6 - 1 : 0) * 2048>(2 * (HF) + s2)); } while (0)
  SCHED();
  B1ALL(1);
  STAMP(5);
  asm volatile("s_waitcnt vmcnt(9)" : "+v"(in0), "+v"(in1), "+v"(in2), "+v"(pf_dummy)::"memory");
  bar();
  STAMP(6);
  asm volatile("" : "+s"(tag));
  tag += 0x9E3779B97F4A7C15ull;
  const unsigned tagl = (unsigned)tag, tagh = (unsigned)(tag >> 32);
  Disc D; D.ld = bf2f(in0); D.a = bf2f(in1); D.b = bf2f(in2);
#define B1RD2(KC, HF, X) do { B1RD(KC, HF, 2 * (X)); B1RD(KC, HF, 2 * (X) + 1); } while (0)
  B1RD2(0, 0, 0); B1RD2(0, 0, 1); B1RD2(0, 0, 2); B1RD2(0, 0, 3); B1RD2(0, 0, 4); B1RD2(0, 0, 5);
  SCHED();
#define B1GAP(KC, HF, X, RKC, RD, ISS) do { \
    if ((KC) == 0 && (HF) == 0 && (X) < 5) mfma32z(acc[(X) % 5], fa[HF][(X) / 5], fb[HF][(X) / 5][(X) % 5]); \
    else mfma32(acc[(X) % 5], fa[HF][(X) / 5], fb[HF][(X) / 5][(X) % 5]); \
    if (RD) { if ((X) == 0) B1RD2(RKC, 1 - (HF), 0); if ((X) == 1) B1RD2(RKC, 1 - (HF), 1); if ((X) == 3) B1RD2(RKC, 1 - (HF), 2); \
              if ((X) == 4) B1RD2(RKC, 1 - (HF), 3); if ((X) == 6) B1RD2(RKC, 1 - (HF), 4); if ((X) == 7) B1RD2(RKC, 1 - (HF), 5); } \
    if ((KC) == 0 && (HF) == 0) { if ((X) >= 1) B1ISSUE(2, ((X) >= 1 ? (X) - 1 : 0)); } \
    else { if ((X) == 2) { ISS(0); } if ((X) == 5) { ISS(1); } if ((X) == 8) { ISS(2); } if ((X) == 9) { ISS(3); } if ((X) == 0) { ISS(4); } } \
    if ((KC) >= 1 && (KC) <= 3) { constexpr int ds_ = ((KC) - 1) * 12 + (HF) * 6 + ((X) == 0 ? 0 : (X) == 1 ? 1 : (X) == 3 ? 2 : (X) == 4 ? 3 : (X) == 6 ? 4 : (X) == 7 ? 5 : -100); \
      if constexpr (ds_ >= 0 && ds_ <= 28) disc_slice<ds_>(D, lds, plc); } \
    SCHED(); } while (0)
#define B1HALF(KC, HF, RKC, RD, ISS) do { B1GAP(KC, HF, 0, RKC, RD, ISS); B1GAP(KC, HF, 1, RKC, RD, ISS); B1GAP(KC, HF, 2, RKC, RD, ISS); B1GAP(KC, HF, 3, RKC, RD, ISS); \
    B1GAP(KC, HF, 4, RKC, RD, ISS); B1GAP(KC, HF, 5, RKC, RD, ISS); B1GAP(KC, HF, 6, RKC, RD, ISS); B1GAP(KC, HF, 7, RKC, RD, ISS); B1GAP(KC, HF, 8, RKC, RD, ISS); B1GAP(KC, HF, 9, RKC, RD, ISS); } while (0)
#define B1STEP(KC, ISSA, VM, ISSB) do { B1HALF(KC, 0, KC, true, ISSA); if ((KC) < 9) { vmwait<VM>(); bar(); } B1HALF(KC, 1, ((KC) < 9 ? (KC) + 1 : 0), ((KC) < 9), ISSB); } while (0)
#define IA(KC, n) do { if ((n) < 4) B1ISSUE((KC) + 2, 5 + ((n) & 3)); } while (0)
#define IB(KC, n) B1ISSUE((KC) + 3, ((n) + 1) % 5)
#define IA0(n) IA(0, n)
#define IA1(n) IA(1, n)
#define IA2(n) IA(2, n)
#define IA3(n) IA(3, n)
#define IA4(n) IA(4, n)
#define IA5(n) IA(5, n)
#define IA6(n) IA(6, n)
#define IA7(n) IA(7, n)
#define IB0(n) IB(0, n)
#define IB1(n) IB(1, n)
#define IB2(n) IB(2, n)
#define IB3(n) IB(3, n)
#define IB4(n) IB(4, n)
#define IB5(n) IB(5, n)
#define IB6(n) IB(6, n)
#define IC0(n) B2ISSUE(0, ((n) + 1) % 5)
#define IC1(n) B2ISSUE(1, ((n) + 1) % 5)
#define IC2(n) B2ISSUE(2, ((n) + 1) % 5)
#define INONE(n) do {} while (0)
  B1STEP(0, IA0, 9, IB0); B1STEP(1, IA1, 9, IB1); B1STEP(2, IA2, 9, IB2); B1STEP(3, IA3, 9, IB3); B1STEP(4, IA4, 9, IB4);
  B1STEP(5, IA5, 9, IB5); B1STEP(6, IA6, 9, IB6); B1STEP(7, IA7, 9, IC0); B1STEP(8, INONE, 5, IC1); B1STEP(9, IC2, 0, INONE);
  STAMP(7);
  const int part = lane & 1, sub = 2 * w + h, pl0 = i32 >> 1;
  float la[5], lb[5], ko[5], kx[5], k1[5], k2[5], cfr[5], cfi[5];
#pragma unroll
  for (int n = 0; n < 5; ++n) {
    const f32x4 pp = *(LAS f32x4*)(lds + T_PAR + (pl0 + 16 * n) * 32);
    la[n] = pp[0]; lb[n] = part ? pp[1] : -pp[1];
    cfr[n] = pp[2]; cfi[n] = pp[3];
  }
  asm volatile("s_nop 7\n\ts_nop 3" : "+v"(acc[0]), "+v"(acc[1]), "+v"(acc[2]), "+v"(acc[3]), "+v"(acc[4]));
  {
    float s[5] = {acc[0][0], acc[1][0], acc[2][0], acc[3][0], acc[4][0]};
#pragma unroll
    for (int t = 1; t < 16; ++t) {
      float o[5];
      scan_step(o[0], o[1], o[2], o[3], o[4], acc[0][t], acc[1][t], acc[2][t], acc[3][t], acc[4][t], s, la, lb);
#pragma unroll
      for (int n = 0; n < 5; ++n) s[n] = o[n];
    }
#pragma unroll
    for (int n = 0; n < 5; ++n) *(LAS float*)(lds + T_E + (sub * 160 + n * 32 + i32) * 4) = s[n];
  }
  bar();
  STAMP(8);
  const __amdgpu_buffer_rsrc_t rG = mkrs(P.ws + WS_GB1, 128 * 1280);
  unsigned gme = (j * 16 + c) * 1280;
  asm volatile("" : "+s"(gme));
  const f32x4 pq = *(LAS f32x4*)(lds + T_PAR + plc * 32 + 16);
  {
    float hr = 0.f, hi = 0.f;
    f32x2 e[8];
#pragma unroll
    for (int k = 0; k < 8; ++k) e[k] = *(LAS f32x2*)(lds + T_E + (k * 80 + plc) * 8);
    f32x2 lp[8];
#pragma unroll
    for (int k = 0; k < 8; ++k) {
      lp[k] = (f32x2){hr, hi};
      const float t = __builtin_fmaf(pq[2], hr, __builtin_fmaf(-pq[3], hi, e[k][0])); hi = __builtin_fmaf(pq[2], hi, __builtin_fmaf(pq[3], hr, e[k][1])); hr = t;
    }
    const u32x4 gr = {__float_as_uint(hr), tagl, __float_as_uint(hi), tagh};
    if (lane < 20) __builtin_amdgcn_raw_buffer_store_b128(gr, rG, plc * 16, gme, 16);
#pragma unroll
    for (int k = 0; k < 8; ++k) *(LAS f32x2*)(lds + T_L + (k * 80 + plc) * 8) = lp[k];
  }
  STAMP(9);
#pragma unroll
  for (int n = 0; n < 5; ++n) {
    const f32x2 pw = *(LAS f32x2*)(lds + T_POW + (sub * 80 + pl0 + 16 * n) * 8);
    ko[n] = part ? -2.f * cfr[n] : 2.f * cfr[n]; kx[n] = -2.f * cfi[n];
    const float wr_ = 2.f * (cfr[n] * pw[0] - cfi[n] * pw[1]), wi_ = -2.f * (cfr[n] * pw[1] + cfi[n] * pw[0]);
    k1[n] = part ? wi_ : wr_; k2[n] = part ? -wr_ : wi_;
    PIN2(k1[n], k2[n]);
  }
#pragma unroll
  for (int x = 0; x < 20; ++x) {
    const int n = x >> 2, q = (x & 3) * 4;
    float u0 = acc[n][q], u1 = acc[n][q + 1], u2 = acc[n][q + 2], u3 = acc[n][q + 3], t0, t1, t2, t3;
    asm volatile("s_nop 1\n\tv_mul_f32_dpp %4, %0, %8 quad_perm:[1,0,3,2] row_mask:0xf bank_mask:0xf\n\tv_mul_f32_dpp %5, %1, %8 quad_perm:[1,0,3,2] row_mask:0xf bank_mask:0xf\n\t"
                 "v_mul_f32_dpp %6, %2, %8 quad_perm:[1,0,3,2] row_mask:0xf bank_mask:0xf\n\tv_mul_f32_dpp %7, %3, %8 quad_perm:[1,0,3,2] row_mask:0xf bank_mask:0xf\n\t"
                 "v_fma_f32 %0, %0, %9, %4\n\tv_fma_f32 %1, %1, %9, %5\n\tv_fma_f32 %2, %2, %9, %6\n\tv_fma_f32 %3, %3, %9, %7"
                 : "+v"(u0), "+v"(u1), "+v"(u2), "+v"(u3), "=&v"(t0), "=&v"(t1), "=&v"(t2), "=&v"(t3) : "v"(kx[n]), "v"(ko[n]));
    acc[n][q] = u0; acc[n][q + 1] = u1; acc[n][q + 2] = u2; acc[n][q + 3] = u3;
  }
  STAMP(10);
  {
    float hr = 0.f, hi = 0.f;
    u32x4 gv[15];
    for (unsigned sp = 0; sp < SPIN_CAP; ++sp) {
      bool ok = true;
#pragma unroll
      for (int k = 0; k < 15; ++k)
        gv[k] = __builtin_amdgcn_raw_buffer_load_b128(rG, plc * 16, c - 15 + k >= 0 ? gme - (15 - k) * 1280 : 0x7fffff00, 16);
#pragma unroll
      for (int k = 0; k < 15; ++k) ok &= (c - 15 + k < 0) | ((gv[k][1] == tagl) & (gv[k][3] == tagh));
      if (__all(ok)) break;
      asm volatile("" ::: "memory");
    }
    STAMP(11);
#pragma unroll
    for (int k = 0; k < 15; ++k) {
      const float t = __builtin_fmaf(pq[0], hr, __builtin_fmaf(-pq[1], hi, __uint_as_float(gv[k][0])));
      hi = __builtin_fmaf(pq[0], hi, __builtin_fmaf(pq[1], hr, __uint_as_float(gv[k][2]))); hr = t;
    }
    *(LAS f32x2*)(lds + T_H + plc * 8) = (f32x2){hr, hi};
  }
  bar();
  STAMP(12);
  float s[5];
#pragma unroll
  for (int n = 0; n < 5; ++n) {
    const f32x2 L = *(LAS f32x2*)(lds + T_L + (sub * 80 + pl0 + 16 * n) * 8);
    const f32x2 H = *(LAS f32x2*)(lds + T_H + (pl0 + 16 * n) * 8);
    const float lg = part ? -2.f * (cfr[n] * L[1] + cfi[n] * L[0]) : 2.f * (cfr[n] * L[0] - cfi[n] * L[1]);
    s[n] = lg + k1[n] * H[0] + k2[n] * H[1];
    lb[n] = -lb[n];
  }
  bar();
#pragma unroll
  for (int t = 0; t < 16; ++t) {
    float o[5];
    scan_step(o[0], o[1], o[2], o[3], o[4], acc[0][t], acc[1][t], acc[2][t], acc[3][t], acc[4][t], s, la, lb);
#pragma unroll
    for (int n = 0; n < 5; ++n) { s[n] = o[n]; acc[n][t] = o[n]; }
    PIN1(s[0]);
    if (t == 0) B2ISSUE(3, 0); if (t == 1) B2ISSUE(3, 1); if (t == 2) B2ISSUE(3, 2); if (t == 3) B2ISSUE(3, 3); if (t == 4) B2ISSUE(3, 4);
    if (t == 5) B2ISSUE(4, 0); if (t == 6) B2ISSUE(4, 1); if (t == 7) B2ISSUE(4, 2); if (t == 8) B2ISSUE(4, 3); if (t == 9) B2ISSUE(4, 4);
    SCHED();
  }
  const unsigned trw = TB + w * 12800 + i32 * 80 + h * 32;
#pragma unroll
  for (int n = 0; n < 5; ++n) {
#pragma unroll
    for (int hh = 0; hh < 2; ++hh) {
      const u32x4 v = {pk2(acc[n][8 * hh], acc[n][8 * hh + 1]), pk2(acc[n][8 * hh + 2], acc[n][8 * hh + 3]),
                       pk2(acc[n][8 * hh + 4], acc[n][8 * hh + 5]), pk2(acc[n][8 * hh + 6], acc[n][8 * hh + 7])};
      *(LAS u32x4*)(lds + trw + n * 2560 + hh * 16) = v;
    }
  }
  const __amdgpu_buffer_rsrc_t rH = mkrs(P.ws + WS_HB + c * 327680, 327680);
  const unsigned vhl = w * 1024 + lane * 16;
  {
    const unsigned trr = TB + w * 12800 + (16 * h + 2 * (i >> 2)) * 80 + (g & 1) * 32 + (i & 3) * 8;
#pragma unroll
    for (int n = 0; n < 5; ++n)
#pragma unroll
      for (int pt = 0; pt < 2; ++pt) {
        const bf16x8 v = cat(ldstr(lds, trr + n * 2560 + pt * 80), ldstr(lds, trr + n * 2560 + pt * 80 + 640));
        __builtin_amdgcn_raw_buffer_store_b128(__builtin_bit_cast(u32x4, v), rH, vhl, (pt * 40 + j * 5 + n) * 4096, 16);
      }
  }
  STAMP(13);
  vmwait<0>(); bar();
  if (w == 0 && lane == 0) __hip_atomic_store((GAS u64*)(P.ws + WS_FB2 + (c * 8 + j) * 128), tag, __ATOMIC_RELAXED, __HIP_MEMORY_SCOPE_AGENT);
  STAMP(14);
  f32x16 y[2];
  f32x4 z[2];
  {
    const unsigned d2 = *(const unsigned*)(P.dsk + j * 80 + 2 * i32);
    const float d0 = bf2f(d2 & 0xffffu), d1 = bf2f(d2 >> 16), d4 = bf2f(P.dsk[j * 80 + 64 + i]);
    const u16* xr = P.Bx + (c * 128 + 32 * w) * 640 + j * 80;
    unsigned xa[16], xb[8];
#pragma unroll
    for (int r = 0; r < 16; ++r) xa[r] = *(const unsigned*)(xr + ((r & 3) + 8 * (r >> 2) + 4 * h) * 640 + 2 * i32);
#pragma unroll
    for (int r = 0; r < 8; ++r) xb[r] = xr[(16 * (r >> 2) + 4 * g + (r & 3)) * 640 + 64 + i];
    SCHED();
    B2ISSUE(5, 0); B2ISSUE(5, 1); B2ISSUE(5, 2); B2ISSUE(5, 3); B2ISSUE(5, 4);
    B2ISSUE(6, 0); B2ISSUE(6, 1); B2ISSUE(6, 2); B2ISSUE(6, 3); B2ISSUE(6, 4);
#pragma unroll
    for (int r = 0; r < 16; ++r) { y[0][r] = rbf(bf2f(xa[r] & 0xffffu) * d0); y[1][r] = rbf(bf2f(xa[r] >> 16) * d1); }
#pragma unroll
    for (int r = 0; r < 8; ++r) z[r >> 2][r & 3] = rbf(bf2f(xb[r]) * d4);
    asm volatile("" : "+v"(y[0]), "+v"(y[1]), "+v"(z[0]), "+v"(z[1]));
  }
  STAMP(15);
  poll_flags(P.ws + WS_FB2 + c * 1024, 128, 8, lane, tag);
  STAMP(16);
  if (c == 15 && j == 0 && w == 0 && lane == 0) __hip_atomic_store((GAS u64*)(P.ws + WS_EPOCH_B), tag, __ATOMIC_RELAXED, __HIP_MEMORY_SCOPE_AGENT);
#define HAUX 16
#define XPD 6
  constexpr int PD = XPD;
  u32x4 hf_[PD][2];
  unsigned kc3 = rot * 128;
#define B2HLD(KS) do { const unsigned so_ = ((((KS) >> 1) & 1) * 40 + ((KS) & 1) * 2) * 4096 + kc3 * 128; \
                       hf_[(KS) % PD][0] = __builtin_amdgcn_raw_buffer_load_b128(rH, vhl, so_, HAUX); \
                       hf_[(KS) % PD][1] = __builtin_amdgcn_raw_buffer_load_b128(rH, vhl, so_ + 4096, HAUX); if (((KS) & 3) == 3) kadv(kc3); } while (0)
#pragma unroll
  for (int ks = 0; ks < PD - 1; ++ks) B2HLD(ks);
  LBase<6, 3> aC;
#pragma unroll
  for (int s = 0; s < 4; ++s) aC.v[0][s] = i32 * 128 + (((2 * s + h) ^ ma) << 4);
#pragma unroll
  for (int s = 0; s < 2; ++s) aC.v[0][4 + s] = (64 + i) * 128 + (((4 * s + 2 * (g & 1) + (g >> 1)) ^ (i >> 1)) << 4);
  aC.fin();
  bf16x8 fc[2][5];
#define B2RD(KS, N) fc[(KS) & 1][N] = ldsr128(lds, aC.at<cbase((KS) >> 2, ((KS) >> 1) & 1) + ((N) < 4 ? ((N) & 1) * 4096 : 0)>((N) < 4 ? 2 * ((KS) & 1) + ((N) >> 1) : 4 + ((KS) & 1)))
  B2RD(0, 0); B2RD(0, 1); B2RD(0, 2); B2RD(0, 3); B2RD(0, 4);
  SCHED();
#define B2LATE(N) do { if constexpr ((N) >= 0) B2ISSUE(7 + ((N) < 0 ? 0 : (N)) / 5, ((N) < 0 ? 0 : (N)) % 5); } while (0)
#define X2 0
#define B2HLD1(KS, Q) do { if constexpr ((KS) < 40 && !(X2 & 1)) { const unsigned so_ = ((((KS) >> 1) & 1) * 40 + ((KS) & 1) * 2 + (Q)) * 4096 + kc3 * 128; \
                       hf_[(KS) % PD][Q] = __builtin_amdgcn_raw_buffer_load_b128(rH, vhl, so_, HAUX); if (((KS) & 3) == 3 && (Q) == 1) kadv(kc3); } } while (0)
#define B2RDX(KS, N) do { if constexpr ((KS) < 40 && !(X2 & 2)) B2RD(KS, N); } while (0)
#define B2STEP(KS) do { \
    if ((KS) == 27) vmwait<10>(); \
    if ((KS) == 11 || (KS) == 27) bar();   \
    u32x4 f0 = hf_[(KS) % PD][0], f1 = hf_[(KS) % PD][1]; \
    mfma32(y[0], __builtin_bit_cast(bf16x8, f0), fc[(KS) & 1][0]); \
    B2RDX((KS) + 1, 0); B2RDX((KS) + 1, 1); B2RDX((KS) + 1, 2); \
    SCHED(); \
    mfma32(y[1], __builtin_bit_cast(bf16x8, f0), fc[(KS) & 1][1]); \
    B2HLD1((KS) + PD - 1, 0); \
    SCHED(); \
    mfma32(y[0], __builtin_bit_cast(bf16x8, f1), fc[(KS) & 1][2]); \
    B2HLD1((KS) + PD - 1, 1); \
    SCHED(); \
    mfma32(y[1], __builtin_bit_cast(bf16x8, f1), fc[(KS) & 1][3]); \
    if constexpr (!(X2 & 4)) swap16x4(f0, f1); \
    B2RDX((KS) + 1, 3); B2RDX((KS) + 1, 4); \
    SCHED(); \
    mfma16x2(z[0], z[1], f0, f1, fc[(KS) & 1][4]); \
    B2LATE(late_dma(KS, 0)); B2LATE(late_dma(KS, 1)); \
    SCHED(); } while (0)
#define B2STEP4(K) do { B2STEP(K); B2STEP(K + 1); B2STEP(K + 2); B2STEP(K + 3); } while (0)
  B2STEP4(0); B2STEP4(4); B2STEP4(8); B2STEP4(12); B2STEP4(16); B2STEP4(20); B2STEP4(24); B2STEP4(28); B2STEP4(32); B2STEP4(36);
  STAMP(17);
  asm volatile("s_nop 7\n\ts_nop 3" : "+v"(y[0]), "+v"(y[1]), "+v"(z[0]), "+v"(z[1]));
  {
    const __amdgpu_buffer_rsrc_t rO = mkrs(P.out, 2048 * 5120);
    const unsigned so = (c * 128 + 32 * w) * 5120 + (640 + j * 80) * 4;
#pragma unroll
    for (int r = 0; r < 16; ++r) {
      const u32x2 v = {__float_as_uint(y[0][r]), __float_as_uint(y[1][r])};
      __builtin_amdgcn_raw_buffer_store_b64(v, rO, h * 20480 + i32 * 8, so + ((r & 3) + 8 * (r >> 2)) * 5120, 16);
    }
#pragma unroll
    for (int r = 0; r < 8; ++r)
      __builtin_amdgcn_raw_buffer_store_b32(__float_as_uint(z[r >> 2][r & 3]), rO, g * 20480 + 256 + i * 4, so + (16 * (r >> 2) + (r & 3)) * 5120, 16);
  }
  CODE_END(".Lcb_end");
  vmwait<0>();
  STAMP(18);
}

constexpr int RINGA = 36928, A_Q0 = RINGA, A_R1 = 118848, A_WPHI = 151616, A_KVT = 154816, A_ZERO = 161536, A_ONES = 163072;
constexpr int qbase(int qc) { return qc < 4 ? A_Q0 + qc * 20480 : 0; }
DI void mfma32a(f32x16& c, bf16x8 a, bf16x8 b) { asm volatile("v_mfma_f32_32x32x16_bf16 %0, %1, %2, %0" : "+v"(c) : "v"(a), "a"(b)); }
DI void mfma32az(f32x16& c, bf16x8 a, bf16x8 b) { asm volatile("v_mfma_f32_32x32x16_bf16 %0, %1, %2, 0" : "=&v"(c) : "v"(a), "a"(b)); }
DI void mfma32fa(f32x16& c, bf16x8 a, u32x4 b) { asm volatile("v_mfma_f32_32x32x16_bf16 %0, %1, %2, %0" : "+v"(c) : "a"(a), "v"(b)); }
DI void mfma32faz(f32x16& c, bf16x8 a, u32x4 b) { asm volatile("v_mfma_f32_32x32x16_bf16 %0, %1, %2, 0" : "=&v"(c) : "a"(a), "v"(b)); }
DI void mfma16z(f32x4& c, bf16x8 a, bf16x8 b) { asm volatile("v_mfma_f32_16x16x32_bf16 %0, %1, %2, 0" : "=&v"(c) : "v"(a), "v"(b)); }
#define NOP12(...) asm volatile("s_nop 7\n\ts_nop 3" : __VA_ARGS__)
DI u32x4 pk8(const f32x16& a, int o) { return (u32x4){pk2(a[o], a[o + 1]), pk2(a[o + 2], a[o + 3]), pk2(a[o + 4], a[o + 5]), pk2(a[o + 6], a[o + 7])}; }
DI u32x4 pk44(const f32x16& a, int oa, const f32x16& b, int ob) { return (u32x4){pk2(a[oa], a[oa + 1]), pk2(a[oa + 2], a[oa + 3]), pk2(b[ob], b[ob + 1]), pk2(b[ob + 2], b[ob + 3])}; }
constexpr int fidx(int T, int u) { return T == 0 ? (u == 4 ? 3 : u) : T == 1 ? 3 + u : 8 + u; }
constexpr bool isfirst(int kh, int m0) { return kh == 0 ? m0 < 5 : (m0 < 3 || m0 >= 8); }

DI void dual_softmax(const float (&p)[20], u32x4 (&o)[5]) {
  constexpr float L2E = 1.4426950408889634f;
  float mx = p[0], mn = p[0];
#pragma unroll
  for (int k = 1; k < 20; ++k) { mx = fmaxf(mx, p[k]); mn = fminf(mn, p[k]); }
  const float cx = -mx * L2E, cn = mn * L2E;
  float e[40], sp = 0.f, sn = 0.f;
#pragma unroll
  for (int k = 0; k < 20; ++k) {
    e[k] = __builtin_amdgcn_exp2f(__builtin_fmaf(p[k], L2E, cx));
    e[20 + k] = __builtin_amdgcn_exp2f(__builtin_fmaf(p[k], -L2E, cn));
    sp += e[k]; sn += e[20 + k];
  }
  const float rp = __builtin_amdgcn_rcpf(sp), rn = __builtin_amdgcn_rcpf(sn);
#pragma unroll
  for (int k = 0; k < 20; ++k) { e[k] *= rp; e[20 + k] *= rn; }
#pragma unroll
  for (int u = 0; u < 5; ++u) o[u] = (u32x4){pk2(e[8 * u], e[8 * u + 1]), pk2(e[8 * u + 2], e[8 * u + 3]), pk2(e[8 * u + 4], e[8 * u + 5]), pk2(e[8 * u + 6], e[8 * u + 7])};
}
DI void feature_mfma(const f32x16& a0, const f32x16& a1, const f32x16& a2, const bf16x8 (&wf)[13], f32x16 (&pp)[3]) {
  u32x4 kb[5] = {pk8(a0, 0), pk8(a0, 8), pk8(a1, 0), pk8(a1, 8), pk8(a2, 0)};
  asm volatile("s_nop 1" : "+v"(kb[0]), "+v"(kb[1]), "+v"(kb[2]), "+v"(kb[3]), "+v"(kb[4]));
  mfma32faz(pp[0], wf[fidx(0, 0)], kb[0]); mfma32faz(pp[1], wf[fidx(1, 1)], kb[1]); mfma32faz(pp[2], wf[fidx(2, 0)], kb[0]);
  mfma32fa(pp[0], wf[fidx(0, 1)], kb[1]); mfma32fa(pp[1], wf[fidx(1, 2)], kb[2]); mfma32fa(pp[2], wf[fidx(2, 1)], kb[1]);
  mfma32fa(pp[0], wf[fidx(0, 2)], kb[2]); mfma32fa(pp[1], wf[fidx(1, 3)], kb[3]); mfma32fa(pp[2], wf[fidx(2, 2)], kb[2]);
  mfma32fa(pp[2], wf[fidx(2, 3)], kb[3]);
  mfma32fa(pp[0], wf[fidx(0, 4)], kb[4]); mfma32fa(pp[2], wf[fidx(2, 4)], kb[4]); mfma32fa(pp[1], wf[fidx(1, 4)], kb[4]);
  NOP12("+v"(pp[0]), "+v"(pp[1]), "+v"(pp[2]));
}
template <int HD> DI void head_vals(const f32x16 (&pp)[3], float (&p)[20]) {
#pragma unroll
  for (int k = 0; k < 16; ++k) p[k] = pp[HD][k];
#pragma unroll
  for (int k = 0; k < 4; ++k) p[16 + k] = pp[2][4 * HD + k];
}

DI void chain_a(const Params& P, LAS char* lds, int c, int G, int rot, int sh, int w, int lane PROF_ARG) {
  CODE_PREFETCH(".Lca_beg", ".Lca_end");
  const int g = lane >> 4, i = lane & 15, h = lane >> 5, i32 = lane & 31, e3 = lane >> 3, pos = lane & 7, q = i >> 2, p = i & 3;
  const u32x4 sX = mksrd(P.Ax, 2048 * 1280), sW = mksrd(P.wqkv, 640 * 3840);
  const unsigned vx0 = e3 * 1280 + ((pos ^ (e3 >> 1)) << 4), vx1 = e3 * 1280 + ((pos ^ (4 + (e3 >> 1))) << 4);
  unsigned pk_[5], pu_[5], vW[5];
#pragma unroll
  for (int u = 0; u < 5; ++u) {
    const unsigned U = 64 * u + lane, pr = (U * 6554u) >> 16;
    pu_[u] = U - 10 * pr; pk_[u] = (pr & ~7u) | ((pr & 1u) << 2) | ((pr & 7u) >> 1);
    vW[u] = pk_[u] * 3840 + pu_[u] * 16;
  }
  const unsigned sx0 = (c * 128 + 32 * w) * 1280;
  const unsigned swq = (w & 1) * 32 * 3840 + G * 160, sw0 = swq + (1 + (w >> 1)) * 1280;
  const unsigned lbx = w * 4096, lbq = w * 5120, lbw = 16384 + lbq + (w >> 1) * 32;
  unsigned kc1 = rot * 128;
#define A1ISSUE(KC, U) do { constexpr int sl_ = ((KC) % 3) * RINGA; \
    if constexpr ((U) < 4) dma16<sl_ + (U) * 1024, (U) * 10240>(sX, ((U) & 1) ? vx1 : vx0, sx0 + kc1, lbx); \
    else dma16<sl_ + ((U) - 4) * 1024, 0>(sW, vW[(U) >= 4 ? (U) - 4 : 0], sw0 + kc1 * 1920, lbw); \
    if ((U) == 8) kadv(kc1); } while (0)
#define A1ALL(KC) do { A1ISSUE(KC, 0); A1ISSUE(KC, 4); A1ISSUE(KC, 5); A1ISSUE(KC, 1); A1ISSUE(KC, 6); A1ISSUE(KC, 2); A1ISSUE(KC, 7); A1ISSUE(KC, 3); A1ISSUE(KC, 8); } while (0)
  A1ALL(0);
  dma16<A_WPHI, 0>(mksrd(P.wphi, 32 * 800), lane * 16, G * 3200 + w * 1024, w * 1024);
  u64 tag = *(const CAS u64*)(P.ws + WS_EPOCH_A);
  if (w == 0) { *(LAS u32x4*)(lds + A_ZERO + lane * 16) = (u32x4){0u, 0u, 0u, 0u}; if (lane < 32) *(LAS u32x4*)(lds + A_ZERO + 1024 + lane * 16) = (u32x4){0u, 0u, 0u, 0u}; }
  if (w == 1 && lane == 0) *(LAS u32x4*)(lds + A_ONES) = (u32x4){0x3f80u, 0u, 0u, 0u};
  unsigned kq = rot * 128;
  if (w >= 2) kadv(kq);
#define AQISSUE(QC, U) do { dma16<qbase(QC) + (U) * 1024, 0>(sW, vW[U], swq + kq * 1920, lbq); if ((U) == 4) { kadv(kq); kadv(kq); } } while (0)
  LBase<4, 2> aX;
#pragma unroll
  for (int s = 0; s < 4; ++s) aX.v[0][s] = (w * 32 + i32) * 128 + (((2 * s + h) ^ ((i32 >> 1) & 7)) << 4);
  aX.fin();
  LBase<6, 2> aT;
  {
    const unsigned x = 2 * (g & 1) + (p >> 1), hh = p & 1, rowb = (8 * (g >> 1) + 2 * q) * 160;
    aT.v[0][0] = rowb + 8 * (x + 12 * hh);
    aT.v[0][1] = rowb + 8 * (4 + x + 12 * hh);
    aT.v[0][2] = rowb + (x < 2 ? 8 * (8 + x + 2 * hh) : 10272 + 8 * (x - 2 + 10 * hh));
    aT.v[0][3] = rowb + 10272 + 8 * (2 + x + 12 * hh);
    aT.v[0][4] = rowb + 10272 + 8 * (hh ? (x < 2 ? 12 + x : 16 + x) : 6 + x);
    aT.v[0][5] = rowb + 8 * (x < 2 ? 8 + x + 2 * hh : 2 + x + 10 * hh);
  }
  aT.fin();
  SCHED();
  A1ALL(1);
  STAMP(1);
  asm volatile("s_waitcnt vmcnt(9)" : "+v"(pf_dummy)::"memory"); bar();
  STAMP(2);
  asm volatile("" : "+s"(tag));
  tag += 0x9E3779B97F4A7C15ull;
  const unsigned tagl = (unsigned)tag, tagh = (unsigned)(tag >> 32);
  bf16x8 xf[40], fw[4][5];
  f32x16 acc[5];
#define A1RDW(KC, S, T) fw[S][T] = cat(ldstr(lds, aT.at<((KC) % 3) * RINGA + 16384 + (S) * 2560>(T)), ldstr(lds, aT.at<((KC) % 3) * RINGA + 16384 + (S) * 2560 + 160>(T)))
#define A1RDX(KC, S) xf[4 * (KC) + (S)] = ldsr128(lds, aX.at<((KC) % 3) * RINGA>(S))
#define A1RDG(KC, HF, N) do { constexpr int s0_ = 2 * (HF), s1_ = 2 * (HF) + 1; \
    if ((N) == 0) { A1RDX(KC, s0_); A1RDW(KC, s0_, 0); } if ((N) == 1) { A1RDW(KC, s0_, 1); A1RDW(KC, s0_, 2); } if ((N) == 2) { A1RDW(KC, s0_, 3); } \
    if ((N) == 3) { A1RDW(KC, s0_, 4); A1RDX(KC, s1_); } if ((N) == 4) { A1RDW(KC, s1_, 0); } if ((N) == 5) { A1RDW(KC, s1_, 1); A1RDW(KC, s1_, 2); } \
    if ((N) == 6) { A1RDW(KC, s1_, 3); } if ((N) == 7) { A1RDW(KC, s1_, 4); } } while (0)
  A1RDG(0, 0, 0); A1RDG(0, 0, 1); A1RDG(0, 0, 2); A1RDG(0, 0, 3); A1RDG(0, 0, 4); A1RDG(0, 0, 5); A1RDG(0, 0, 6); A1RDG(0, 0, 7);
  bf16x8 wf[13];
  {
    const bool same = (p & 1) == h;
    const unsigned xo = 2 * (g & 1) + (p >> 1);
    const unsigned b01 = A_WPHI + q * 40 + 8 * xo, b2 = A_WPHI + q * 40 + 32;
#define WFADDR(T, M0) ((same && ((T) == 2 ? (xo < 2 && ((xo == 0) == (h ? isfirst(1, M0) : isfirst(0, M0)))) : (h ? isfirst(1, M0) == ((T) == 0) : isfirst(0, M0) == ((T) == 0)))) \
                           ? ((T) == 2 ? b2 : b01) + (h ? ((M0) < 8 ? 1920 : 320) : 0) : (unsigned)A_ZERO)
#define WFBUILD(T, U) wf[fidx(T, U)] = cat(ldstr(lds, WFADDR(T, 2 * (U)) + 320 * (U)), ldstr(lds, WFADDR(T, 2 * (U) + 1) + 320 * (U) + 160))
    WFBUILD(0, 0); WFBUILD(0, 1); WFBUILD(0, 2); WFBUILD(0, 4); WFBUILD(1, 1); WFBUILD(1, 2); WFBUILD(1, 3); WFBUILD(1, 4);
    WFBUILD(2, 0); WFBUILD(2, 1); WFBUILD(2, 2); WFBUILD(2, 3); WFBUILD(2, 4);
#pragma unroll
    for (int k = 0; k < 13; ++k) asm volatile("" : "+a"(wf[k]));
  }
  SCHED();
#define A1GAP(KC, HF, X, RKC, RD, ISS) do { constexpr int s_ = 2 * (HF) + (X) / 5; \
    if ((KC) == 0 && (HF) == 0 && (X) < 5) mfma32az(acc[(X) % 5], fw[s_][(X) % 5], xf[4 * (KC) + s_]); \
    else mfma32a(acc[(X) % 5], fw[s_][(X) % 5], xf[4 * (KC) + s_]); \
    if constexpr (RD && (X) < 8) A1RDG(RKC, 1 - (HF), X); \
    if ((KC) == 0 && (HF) == 0) { if ((X) >= 1) A1ISSUE(2, ((X) >= 1 ? (X) - 1 : 0)); } \
    else { if ((X) == 2) { ISS(0); } if ((X) == 4) { ISS(1); } if ((X) == 6) { ISS(2); } if ((X) == 8) { ISS(3); } if ((X) == 9) { ISS(4); } } \
    SCHED(); } while (0)
#define A1HALF(KC, HF, RKC, RD, ISS) do { A1GAP(KC, HF, 0, RKC, RD, ISS); A1GAP(KC, HF, 1, RKC, RD, ISS); A1GAP(KC, HF, 2, RKC, RD, ISS); A1GAP(KC, HF, 3, RKC, RD, ISS); \
    A1GAP(KC, HF, 4, RKC, RD, ISS); A1GAP(KC, HF, 5, RKC, RD, ISS); A1GAP(KC, HF, 6, RKC, RD, ISS); A1GAP(KC, HF, 7, RKC, RD, ISS); A1GAP(KC, HF, 8, RKC, RD, ISS); A1GAP(KC, HF, 9, RKC, RD, ISS); } while (0)
#define A1STEP(KC, ISSA, VM, ISSB) do { A1HALF(KC, 0, KC, true, ISSA); if ((KC) < 9) vmwait<VM>(); bar(); A1HALF(KC, 1, ((KC) < 9 ? (KC) + 1 : 0), ((KC) < 9), ISSB); } while (0)
#define JA(KC, n) do { if constexpr ((n) < 4) A1ISSUE((KC) + 2, 5 + ((n) & 3)); } while (0)
#define JB(KC, n) A1ISSUE((KC) + 3, n)
#define JA1(n) JA(1, n)
#define JA2(n) JA(2, n)
#define JA3(n) JA(3, n)
#define JA4(n) JA(4, n)
#define JA5(n) JA(5, n)
#define JA6(n) JA(6, n)
#define JA7(n) JA(7, n)
#define JB0(n) JB(0, n)
#define JB1(n) JB(1, n)
#define JB2(n) JB(2, n)
#define JB3(n) JB(3, n)
#define JB4(n) JB(4, n)
#define JB5(n) JB(5, n)
#define JB6(n) JB(6, n)
#define JQ0(n) AQISSUE(0, n)
#define JQ1(n) AQISSUE(1, n)
#define JQ2(n) AQISSUE(2, n)
#define JQ3(n) AQISSUE(3, n)
  A1STEP(0, INONE, 9, JB0); A1STEP(1, JA1, 9, JB1); A1STEP(2, JA2, 9, JB2); A1STEP(3, JA3, 9, JB3); A1STEP(4, JA4, 9, JB4);
  A1STEP(5, JA5, 9, JB5); A1STEP(6, JA6, 9, JB6); A1STEP(7, JA7, 9, JQ0); A1STEP(8, INONE, 5, JQ1); A1STEP(9, JQ2, 0, JQ3);
  STAMP(3);
  NOP12("+v"(acc[0]), "+v"(acc[1]), "+v"(acc[2]), "+v"(acc[3]), "+v"(acc[4]));
  const unsigned rg = (w < 2 ? 0 : A_R1) + (32 * (w & 1) + i32) * 352, rv = (w < 2 ? 0 : A_R1) + 22528 + (32 * (w & 1) + i32) * 160;
  {
    f32x16 pp[3];
    feature_mfma(acc[0], acc[1], acc[2], wf, pp);
    *(LAS u32x4*)(lds + rv + 80 * h) = pk8(acc[2], 8);
    *(LAS u32x4*)(lds + rv + 16 + 96 * h) = pk8(acc[3], 0);
    *(LAS u32x4*)(lds + rv + 32 + 96 * h) = pk8(acc[3], 8);
    *(LAS u32x4*)(lds + rv + 48 + 48 * h) = pk8(acc[4], 0);
    *(LAS u32x4*)(lds + rv + 64 + 80 * h) = pk8(acc[4], 8);
    float pv[20];
    u32x4 ph[5];
    head_vals<0>(pp, pv); dual_softmax(pv, ph);
#pragma unroll
    for (int u = 0; u < 5; ++u) *(LAS u32x4*)(lds + rg + 160 * h + 16 * u) = ph[u];
    head_vals<1>(pp, pv); dual_softmax(pv, ph);
#pragma unroll
    for (int u = 0; u < 5; ++u) *(LAS u32x4*)(lds + rg + 160 * h + 80 + 16 * u) = ph[u];
  }
  bar();
  STAMP(4);
  const __amdgpu_buffer_rsrc_t rP = mkrs(P.ws + WS_KVP + (G * 4 + w) * 65536, 65536);
  {
    const unsigned tr = 16 * (g >> 1) + 4 * (g & 1) + q;
    LBase<1, 3> aP, aV;
    aP.v[0][0] = tr * 352 + 80 * w + 8 * p; aV.v[0][0] = 22528 + tr * 160 + 40 * w + 8 * p;
    aP.fin(); aV.fin();
    const unsigned v1b = p == 1 ? (unsigned)A_ONES : aV.v[0][0] + 32, v1m = p == 1 ? 0u : 1u;
    f32x4 kv[3][2];
#define KVSTEP(KS) do { constexpr int ro_ = ((KS) >> 1) * A_R1, r0_ = 32 * ((KS) & 1); \
      bf16x8 a_[3], b_[2]; \
      a_[0] = cat(ldstr(lds, aP.at<ro_ + r0_ * 352>(0)), ldstr(lds, aP.at<ro_ + (r0_ + 8) * 352>(0))); \
      b_[0] = cat(ldstr(lds, aV.at<ro_ + r0_ * 160>(0)), ldstr(lds, aV.at<ro_ + (r0_ + 8) * 160>(0))); \
      b_[1] = cat(ldstr(lds, v1b + v1m * (ro_ + r0_ * 160)), ldstr(lds, v1b + v1m * (ro_ + (r0_ + 8) * 160))); \
      a_[1] = cat(ldstr(lds, aP.at<ro_ + r0_ * 352 + 32>(0)), ldstr(lds, aP.at<ro_ + (r0_ + 8) * 352 + 32>(0))); \
      a_[2] = cat(ldstr(lds, aP.at<ro_ + r0_ * 352 + 64>(0)), ldstr(lds, aP.at<ro_ + (r0_ + 8) * 352 + 64>(0))); \
      _Pragma("unroll") for (int m = 0; m < 3; ++m) _Pragma("unroll") for (int n = 0; n < 2; ++n) { if ((KS) == 0) mfma16z(kv[m][n], a_[m], b_[n]); else mfma16(kv[m][n], a_[m], b_[n]); } } while (0)
    KVSTEP(0); KVSTEP(1); KVSTEP(2); KVSTEP(3);
    asm volatile("s_nop 7" : "+v"(kv[0][0]), "+v"(kv[0][1]), "+v"(kv[1][0]), "+v"(kv[1][1]), "+v"(kv[2][0]), "+v"(kv[2][1]));
#pragma unroll
    for (int m = 0; m < 3; ++m)
#pragma unroll
      for (int n = 0; n < 2; ++n) {
        const bool ok = (m < 2 || g < 2) && (n == 0 || i <= 4);
        __builtin_amdgcn_raw_buffer_store_b128(__builtin_bit_cast(u32x4, kv[m][n]), rP, ok ? ((4 * m + g) * 21 + 16 * n + i) * 16 : 0x7fffff00, c * 4096, 16);
      }
  }
  vmwait<6>(); bar();
  STAMP(5);
  AQISSUE(4, 0); AQISSUE(4, 1); AQISSUE(4, 2); AQISSUE(4, 3); AQISSUE(4, 4);
  f32x16 aq[3];
  bf16x8 fq[3][3];
  u32x4 hp[4];
#define AQOFF(K) (qbase((K) >> 3) + (((K) >> 2) & 1) * 10240 + ((K) & 3) * 2560)
#define AQRD(K, T) fq[(K) % 3][T] = cat(ldstr(lds, aT.at<AQOFF(K)>((T) == 2 ? 5 : (T))), ldstr(lds, aT.at<AQOFF(K) + 160>((T) == 2 ? 5 : (T))))
  AQRD(0, 0); AQRD(0, 1); AQRD(0, 2); AQRD(1, 0); AQRD(1, 1); AQRD(1, 2);
  SCHED();
#define AQGAP(K, T) do { if ((K) == 0) mfma32az(aq[T], fq[(K) % 3][T], xf[K]); else mfma32a(aq[T], fq[(K) % 3][T], xf[K]); \
    if constexpr ((K) + 2 < 40) AQRD((K) + 2, T); SCHED(); } while (0)
#define AQSTEP(K) do { AQGAP(K, 0); AQGAP(K, 1); AQGAP(K, 2); } while (0)
#define AQSTEP5(K) do { AQSTEP(K); AQSTEP(K + 1); AQSTEP(K + 2); AQSTEP(K + 3); AQSTEP(K + 4); } while (0)
  AQSTEP5(0); AQSTEP5(5); AQSTEP5(10);
  vmwait<5>();
  if (lane == 0) __hip_atomic_store((GAS u64*)(P.ws + WS_FA1 + ((G * 4 + w) * 16 + c) * 128), tag, __ATOMIC_RELAXED, __HIP_MEMORY_SCOPE_AGENT);
  SCHED();
  AQSTEP5(15); AQSTEP5(20); AQSTEP5(25);
  STAMP(6);
  poll_flags(P.ws + WS_FA1 + (G * 4 + w) * 2048, 128, 16, lane, tag);
  bar();
  STAMP(7);
  {
    const unsigned vo = g * 16384 + i * 16;
    const u32x4 sP = mksrd(P.ws + WS_KVP + (G * 4 + w) * 65536, 65536);
    const unsigned s0 = c * 256, s1 = s0 + 4096, s2 = s0 + 8192, s3 = s0 + 12288;
    asm volatile("s_nop 4\n\tbuffer_load_dwordx4 %0, %4, %5, %6 offen sc1\n\tbuffer_load_dwordx4 %1, %4, %5, %7 offen sc1\n\t"
                 "buffer_load_dwordx4 %2, %4, %5, %8 offen sc1\n\tbuffer_load_dwordx4 %3, %4, %5, %9 offen sc1"
                 : "=&v"(hp[0]), "=&v"(hp[1]), "=&v"(hp[2]), "=&v"(hp[3]) : "v"(vo), "s"(sP), "s"(s0), "s"(s1), "s"(s2), "s"(s3) : "memory");
  }
  SCHED();
  AQSTEP5(30); AQSTEP5(35);
  bar();
  STAMP(8);
  NOP12("+v"(aq[0]), "+v"(aq[1]), "+v"(aq[2]));
  f32x16 pq[3];
  feature_mfma(aq[0], aq[1], aq[2], wf, pq);
  const __amdgpu_buffer_rsrc_t rF = mkrs(P.ws + WS_KVF + (G * 4 + w) * 4096, 4096);
  {
    asm volatile("s_waitcnt vmcnt(0)" : "+v"(hp[0]), "+v"(hp[1]), "+v"(hp[2]), "+v"(hp[3])::"memory");
    float sv[4];
#pragma unroll
    for (int k = 0; k < 4; ++k) {
      float v = (__uint_as_float(hp[0][k]) + __uint_as_float(hp[1][k])) + (__uint_as_float(hp[2][k]) + __uint_as_float(hp[3][k]));
      const auto r = __builtin_amdgcn_permlane32_swap(__float_as_uint(v), __float_as_uint(v), false, false);
      const unsigned r0 = r[0], r1 = r[1];
      v = __uint_as_float(r0) + __uint_as_float(r1);
      const auto t = __builtin_amdgcn_permlane16_swap(__float_as_uint(v), __float_as_uint(v), false, false);
      const unsigned t0 = t[0], t1 = t[1];
      sv[k] = __uint_as_float(t0) + __uint_as_float(t1);
    }
    const u32x4 gr = {pk2(sv[0], sv[1]), tagl, pk2(sv[2], sv[3]), tagh};
    __builtin_amdgcn_raw_buffer_store_b128(gr, rF, g == 0 ? i * 16 : 0x7fffff00, c * 256, 16);
  }
  STAMP(9);
  const u32x4 sO = mksrd(P.wo, 640 * 1280);
  unsigned vO[5];
#pragma unroll
  for (int u = 0; u < 5; ++u) vO[u] = pk_[u] * 1280 + pu_[u] * 16;
  const unsigned so0 = ((w + sh) & 3) * 160 * 1280 + G * 160, lbo = w * 25600;
#define AOISSUE(N) dma16<(N) * 1024, ((N) / 5) * 40960>(sO, vO[(N) % 5], so0, lbo)
#define AOISSUE5(N) do { AOISSUE(N); AOISSUE(N + 1); AOISSUE(N + 2); AOISSUE(N + 3); AOISSUE(N + 4); } while (0)
  u32x4 phq[10];
  {
    float pv[20];
    u32x4 ph[5];
    AOISSUE5(0); AOISSUE5(5);
    head_vals<0>(pq, pv); dual_softmax(pv, ph);
#pragma unroll
    for (int u = 0; u < 5; ++u) { phq[u] = ph[u]; asm volatile("" : "+v"(phq[u])); }
    SCHED();
    AOISSUE5(10); AOISSUE5(15);
    head_vals<1>(pq, pv); dual_softmax(pv, ph);
#pragma unroll
    for (int u = 0; u < 5; ++u) { phq[5 + u] = ph[u]; asm volatile("" : "+v"(phq[5 + u])); }
    SCHED();
    AOISSUE5(20);
  }
  STAMP(10);
  {
    u32x4 gv[4];
    for (unsigned sp = 0; sp < SPIN_CAP; ++sp) {
      bool ok = true;
#pragma unroll
      for (int m = 0; m < 4; ++m) gv[m] = __builtin_amdgcn_raw_buffer_load_b128(rF, lane * 16, m * 1024, 16);
#pragma unroll
      for (int m = 0; m < 4; ++m) ok &= (lane + 64 * m >= 210) | ((gv[m][1] == tagl) & (gv[m][3] == tagh));
      if (__all(ok)) break;
      asm volatile("" ::: "memory");
    }
#pragma unroll
    for (int m = 0; m < 4; ++m) {
      const unsigned sl = lane + 64 * m, f4 = (sl * 3121u) >> 16, d = sl - 21 * f4;
      if (sl < 210) *(LAS u32x2*)(lds + A_KVT + w * 1680 + d * 80 + f4 * 8) = (u32x2){gv[m][0], gv[m][2]};
    }
  }
  bar();
  STAMP(11);
  const __amdgpu_buffer_rsrc_t rOb = mkrs(P.ws + WS_OB + c * 163840, 163840);
  const unsigned vol = w * 5120 + lane * 16;
  {
    const bool val = ((i32 >> 2) & 1) == h;
    const unsigned xo = i32 >> 3, e = i32 & 3, hb = A_KVT + 2 * h * 1680;
    const unsigned a0 = val ? hb + (e + 4 * xo) * 80 : (unsigned)A_ZERO, a1 = val ? a0 + 1680 : (unsigned)A_ZERO;
    const unsigned a2 = (val && !(xo & 1)) ? hb + (xo == 0 ? 16 + e : 20) * 80 : (unsigned)A_ZERO;
    const unsigned a3 = (val && (xo & 1)) ? hb + 1680 + (xo == 1 ? 16 + e : 20) * 80 : (unsigned)A_ZERO;
    bf16x8 fa_[4][5];
#pragma unroll
    for (int u = 0; u < 5; ++u) { fa_[0][u] = ldsr128(lds, a0 + 16 * u); fa_[1][u] = ldsr128(lds, a1 + 16 * u); fa_[2][u] = ldsr128(lds, a2 + 16 * u); fa_[3][u] = ldsr128(lds, a3 + 16 * u); }
    f32x16 oo[3];
#pragma unroll
    for (int u = 0; u < 5; ++u) {
      if (u == 0) { mfma32z(oo[0], fa_[0][u], __builtin_bit_cast(bf16x8, phq[u])); mfma32z(oo[1], fa_[1][u], __builtin_bit_cast(bf16x8, phq[5 + u])); mfma32z(oo[2], fa_[2][u], __builtin_bit_cast(bf16x8, phq[u])); }
      else { mfma32(oo[0], fa_[0][u], __builtin_bit_cast(bf16x8, phq[u])); mfma32(oo[1], fa_[1][u], __builtin_bit_cast(bf16x8, phq[5 + u])); mfma32(oo[2], fa_[2][u], __builtin_bit_cast(bf16x8, phq[u])); }
      mfma32(oo[2], fa_[3][u], __builtin_bit_cast(bf16x8, phq[5 + u]));
    }
    NOP12("+v"(oo[0]), "+v"(oo[1]), "+v"(oo[2]));
    const float r0 = __builtin_amdgcn_rcpf(oo[2][8] + 1e-6f), r1 = __builtin_amdgcn_rcpf(oo[2][12] + 1e-6f);
#pragma unroll
    for (int k = 0; k < 16; ++k) { oo[0][k] *= r0; oo[1][k] *= r1; }
#pragma unroll
    for (int k = 0; k < 4; ++k) { oo[2][k] *= r0; oo[2][4 + k] *= r1; }
    const u32x4 of[5] = {pk8(oo[0], 0), pk8(oo[0], 8), pk44(oo[2], 0, oo[1], 0), pk8(oo[1], 4), pk44(oo[1], 12, oo[2], 4)};
#pragma unroll
    for (int u = 0; u < 5; ++u) __builtin_amdgcn_raw_buffer_store_b128(of[u], rOb, vol, G * 20480 + u * 1024, 16);
  }
  STAMP(12);
  vmwait<0>(); bar();
  if (w == 0 && lane == 0) __hip_atomic_store((GAS u64*)(P.ws + WS_FA2 + (c * 8 + G) * 128), tag, __ATOMIC_RELAXED, __HIP_MEMORY_SCOPE_AGENT);
  STAMP(13);
  LBase<3, 2> aO;
  aO.v[0][0] = (40 * (g >> 1) + 2 * q) * 160 + 32 * (g & 1) + 8 * p;
  aO.v[0][1] = (40 * (g >> 1) + 8 * (g & 1) + 2 * q) * 160 + 128 + 8 * p;
  aO.v[0][2] = (40 * (g >> 1) + 48 * (g & 1) + 2 * q) * 160 + 128 + 8 * p;
  aO.fin();
  poll_flags(P.ws + WS_FA2 + c * 1024, 128, 8, lane, tag);
  STAMP(14);
  if (c == 0 && G == 0 && w == 0 && lane == 0) __hip_atomic_store((GAS u64*)(P.ws + WS_EPOCH_A), tag, __ATOMIC_RELAXED, __HIP_MEMORY_SCOPE_AGENT);
  u32x4 ofr[40];
#pragma unroll
  for (int S = 0; S < 40; ++S) ofr[S] = __builtin_amdgcn_raw_buffer_load_b128(rOb, vol, ((S / 5 + 2 * sh) & 7) * 20480 + (S % 5) * 1024, 16);
  f32x16 y[2];
  f32x4 z[2] = {(f32x4){0.f, 0.f, 0.f, 0.f}, (f32x4){0.f, 0.f, 0.f, 0.f}};
  bf16x8 fo[2][5];
#define AOOFF(S) ((80 * ((S) / 5) + 8 * ((S) % 5)) * 160)
#define A2RD(M, N) fo[(M) & 1][N] = (N) < 4 ? cat(ldstr(lds, aO.at<AOOFF(2 * (M) + ((N) >> 1 & 1)) + ((N) & 1) * 64>(0)), ldstr(lds, aO.at<AOOFF(2 * (M) + ((N) >> 1 & 1)) + ((N) & 1) * 64 + 160>(0))) \
                                            : cat(ldstr(lds, aO.at<AOOFF(2 * (M))>((2 * (M)) % 5 == 4 ? 2 : 1)), ldstr(lds, aO.at<AOOFF(2 * (M)) + 160>((2 * (M)) % 5 == 4 ? 2 : 1)))
#define A2RDX(M, N) do { if constexpr ((M) < 20) A2RD(M, N); } while (0)
  A2RD(0, 0); A2RD(0, 1); A2RD(0, 2); A2RD(0, 3); A2RD(0, 4);
  SCHED();
#define A2STEP(M) do { u32x4 f0 = ofr[2 * (M)], f1 = ofr[2 * (M) + 1]; \
    if ((M) == 0) mfma32z(y[0], __builtin_bit_cast(bf16x8, f0), fo[(M) & 1][0]); else mfma32(y[0], __builtin_bit_cast(bf16x8, f0), fo[(M) & 1][0]); \
    A2RDX((M) + 1, 0); SCHED(); \
    if ((M) == 0) mfma32z(y[1], __builtin_bit_cast(bf16x8, f0), fo[(M) & 1][1]); else mfma32(y[1], __builtin_bit_cast(bf16x8, f0), fo[(M) & 1][1]); \
    A2RDX((M) + 1, 1); A2RDX((M) + 1, 2); SCHED(); \
    mfma32(y[0], __builtin_bit_cast(bf16x8, f1), fo[(M) & 1][2]); \
    A2RDX((M) + 1, 3); A2RDX((M) + 1, 4); SCHED(); \
    mfma32(y[1], __builtin_bit_cast(bf16x8, f1), fo[(M) & 1][3]); \
    swap16x4(f0, f1); SCHED(); \
    mfma16x2(z[0], z[1], f0, f1, fo[(M) & 1][4]); SCHED(); } while (0)
#define A2STEP4(M) do { A2STEP(M); A2STEP(M + 1); A2STEP(M + 2); A2STEP(M + 3); } while (0)
  A2STEP4(0); A2STEP4(4); A2STEP4(8); A2STEP4(12); A2STEP4(16);
  STAMP(15);
  NOP12("+v"(y[0]), "+v"(y[1]), "+v"(z[0]), "+v"(z[1]));
  {
    const __amdgpu_buffer_rsrc_t rO = mkrs(P.out, 2048 * 5120);
    const unsigned so = (c * 128 + 32 * w) * 5120 + G * 320;
#pragma unroll
    for (int r = 0; r < 16; ++r) {
      const unsigned v = pk2(y[0][r], y[1][r]);
      __builtin_amdgcn_raw_buffer_store_b32(v << 16, rO, h * 20480 + i32 * 4, so + ((r & 3) + 8 * (r >> 2)) * 5120, 16);
      __builtin_amdgcn_raw_buffer_store_b32(v & 0xffff0000u, rO, h * 20480 + 128 + i32 * 4, so + ((r & 3) + 8 * (r >> 2)) * 5120, 16);
    }
#pragma unroll
    for (int r = 0; r < 4; ++r) {
      const unsigned v = pk2(z[0][r], z[1][r]);
      __builtin_amdgcn_raw_buffer_store_b32(v << 16, rO, g * 20480 + 256 + i * 4, so + r * 5120, 16);
      __builtin_amdgcn_raw_buffer_store_b32(v & 0xffff0000u, rO, g * 20480 + 256 + i * 4, so + (16 + r) * 5120, 16);
    }
  }
  CODE_END(".Lca_end");
  vmwait<0>();
  STAMP(16);
}

__global__ __launch_bounds__(256, 1) void fused_kernel(Params P) {
  __shared__ __attribute__((aligned(16))) char smem[163840];
  LAS char* lds = (LAS char*)smem;
  const int b = blockIdx.x, xcd = b & 7, loc = b >> 3, l = loc & 15;
  const int c = (xcd >> 1) * 4 + (l & 3), j = (xcd & 1) * 4 + (l >> 2);
  const int rot = ((((l & 3) + (l >> 2)) & 3) * 5) >> 1;
  const int w = __builtin_amdgcn_readfirstlane(threadIdx.x >> 6), lane = threadIdx.x & 63;
  Prof pf;
#pragma unroll
  for (int k = 0; k < PROF_N; ++k) { pf.r[k] = 0; pf.c[k] = 0; }
  STAMP(0);
  if (b < 8 && w == 0) { STAMP(18); asm volatile("buffer_wbl2 sc1\n\ts_waitcnt vmcnt(0)" ::: "memory"); STAMP(19); }
  if (loc >= 16) chain_b(P, lds, c, j, rot, w, lane PROF_PASS);
  else chain_a(P, lds, c, j, rot, l >> 2, w, lane PROF_PASS);
  if (b == DIAG_B && w == 0) {
    const unsigned dl = (pf.r[DIAG_K] - pf.r[DIAG_K0]) * 4u;
    u64 t0_, t1_;
    asm volatile("s_memrealtime %0\n\ts_waitcnt lgkmcnt(0)" : "=s"(t0_)::"memory");
    do { asm volatile("s_memrealtime %0\n\ts_waitcnt lgkmcnt(0)" : "=s"(t1_)::"memory"); } while ((unsigned)t1_ - (unsigned)t0_ < dl && dl < 100000u);
  }
}

extern "C" void kernel_launch(void* const* d_in, const int* in_sizes, int n_in, void* d_out, int out_size, void* d_ws,
                              size_t ws_size, hipStream_t stream) {
  Params P;
  P.Ax = (const u16*)d_in[0]; P.wqkv = (const u16*)d_in[1]; P.wphi = (const u16*)d_in[2]; P.wo = (const u16*)d_in[3];
  P.Bx = (const u16*)d_in[4]; P.lre = (const u16*)d_in[5]; P.lim = (const u16*)d_in[6]; P.bre = (const u16*)d_in[7];
  P.bim = (const u16*)d_in[8]; P.cre = (const u16*)d_in[9]; P.cim = (const u16*)d_in[10]; P.logdt = (const u16*)d_in[11];
  P.dsk = (const u16*)d_in[12]; P.out = (float*)d_out; P.ws = (char*)d_ws;
  fused_kernel<<<256, 256, 0, stream>>>(P);
}
```

```cpp
#include <hip/hip_runtime.h>
#include <hip/hip_bf16.h>
#include <stdint.h>

typedef unsigned short u16;
typedef unsigned long long u64;
typedef __bf16 bf16x8 __attribute__((ext_vector_type(8)));
typedef __bf16 bf16x4 __attribute__((ext_vector_type(4)));
typedef __bf16 bf16x2 __attribute__((ext_vector_type(2)));
typedef float f32x4 __attribute__((ext_vector_type(4)));
typedef float f32x2 __attribute__((ext_vector_type(2)));
typedef unsigned u32x4 __attribute__((ext_vector_type(4)));
typedef unsigned u32x2 __attribute__((ext_vector_type(2)));
#define LAS __attribute__((address_space(3)))
#define GAS __attribute__((address_space(1)))
#define CAS __attribute__((address_space(4)))
#define DI __device__ __forceinline__
#define SCHED() __builtin_amdgcn_sched_barrier(0)
typedef float f32x16 __attribute__((ext_vector_type(16)));

constexpr unsigned WS_EPOCH_A = 0, WS_EPOCH_B = 256;
constexpr unsigned WS_FA2 = 4096;
constexpr unsigned WS_FB2 = WS_FA2 + 512 * 128;
constexpr unsigned WS_GB1 = WS_FB2 + 512 * 128;
constexpr unsigned WS_KVF = WS_GB1 + 128 * 1280;
constexpr unsigned WS_KVP = WS_KVF + 32 * 4096;
constexpr unsigned WS_OB = WS_KVP + 512 * 8192;
constexpr unsigned WS_HB = WS_OB + 16 * 163840;
constexpr unsigned WS_END = WS_HB + 16 * 327680;
constexpr unsigned SPIN_CAP = 1u << 17;

struct Params {
  const u16 *Ax, *wqkv, *wphi, *wo, *Bx, *lre, *lim, *bre, *bim, *cre, *cim, *logdt, *dsk;
  float* out;
  char* ws;
};

DI float bf2f(unsigned v) { return __uint_as_float(v << 16); }
DI float rbf(float f) { return (float)(__bf16)f; }
DI unsigned pk2(float a, float b) { f32x2 f = {a, b}; return __builtin_bit_cast(unsigned, __builtin_convertvector(f, bf16x2)); }
DI u32x4 mksrd(const void* p, unsigned bytes) {
  const u64 a = (u64)p;
  u32x4 r = {(unsigned)a, (unsigned)(a >> 32) & 0xffffu, bytes, 0x00020000u};
  return r;
}
DI __amdgpu_buffer_rsrc_t mkrs(const void* p, unsigned bytes) { return __builtin_amdgcn_make_buffer_rsrc((void*)p, 0, bytes, 0x00020000); }
template <int LI, int SI>
DI void dma16(u32x4 srd, unsigned vo, unsigned so, unsigned lb) {
  unsigned t;
  asm volatile("s_add_u32 m0, %[lb], %[li]\n\ts_add_u32 %[t], %[so], %[si]\n\tbuffer_load_dwordx4 %[vo], %[srd], %[t] offen lds"
               : [t] "=&s"(t) : [lb] "s"(lb), [li] "i"(LI), [so] "s"(so), [si] "i"(SI), [vo] "v"(vo), [srd] "s"(srd) : "memory", "scc");
}
template <int LI>
DI void dma16s(u32x4 srd, unsigned vo, unsigned so, unsigned lb) {
  asm volatile("s_add_u32 m0, %[lb], %[li]\n\ts_nop 0\n\tbuffer_load_dwordx4 %[vo], %[srd], %[so] offen lds"
               :: [lb] "s"(lb), [li] "i"(LI), [so] "s"(so), [vo] "v"(vo), [srd] "s"(srd) : "memory", "scc");
}
DI void kadv(unsigned& k) { asm volatile("s_add_u32 %0, %0, 0x80\n\ts_cmp_eq_u32 %0, 0x500\n\ts_cselect_b32 %0, 0, %0" : "+s"(k)::"scc"); }
DI void mfma32(f32x16& c, bf16x8 a, bf16x8 b) { asm volatile("v_mfma_f32_32x32x16_bf16 %0, %1, %2, %0" : "+v"(c) : "v"(a), "v"(b)); }
DI void mfma32z(f32x16& c, bf16x8 a, bf16x8 b) { asm volatile("v_mfma_f32_32x32x16_bf16 %0, %1, %2, 0" : "=&v"(c) : "v"(a), "v"(b)); }
DI void mfma16(f32x4& c, bf16x8 a, bf16x8 b) { asm volatile("v_mfma_f32_16x16x32_bf16 %0, %1, %2, %0" : "+v"(c) : "v"(a), "v"(b)); }
DI void swap16x4(u32x4& a, u32x4& b) {
#pragma unroll
  for (int k = 0; k < 4; ++k) {
    const auto r = __builtin_amdgcn_permlane16_swap(a[k], b[k], false, false);
    const unsigned r0 = r[0], r1 = r[1];
    a[k] = r0; b[k] = r1;
  }
}
DI void mfma16x2(f32x4& c0, f32x4& c1, u32x4 a0, u32x4 a1, bf16x8 b) {
  asm volatile("s_nop 1\n\tv_mfma_f32_16x16x32_bf16 %0, %2, %4, %0\n\tv_mfma_f32_16x16x32_bf16 %1, %3, %4, %1" : "+v"(c0), "+v"(c1) : "v"(a0), "v"(a1), "v"(b));
}
template <int N, int NW> struct LBase {
  unsigned v[NW][N];
  DI void fin() {
#pragma unroll
    for (int k = 1; k < NW; ++k)
#pragma unroll
      for (int n = 0; n < N; ++n) { v[k][n] = v[0][n] + k * 65536; asm volatile("" : "+v"(v[k][n])); }
  }
  template <int OFF> DI unsigned at(int n) const { return v[OFF >> 16][n] + (OFF & 65535); }
};
template <int N> DI void vmwait() { asm volatile("s_waitcnt vmcnt(%0)" ::"i"(N) : "memory"); }
DI void bar() { asm volatile("s_waitcnt lgkmcnt(0)\n\ts_barrier" ::: "memory"); }
DI float dppx(float x) { return __builtin_bit_cast(float, __builtin_amdgcn_update_dpp(0, __builtin_bit_cast(int, x), 0xB1, 0xF, 0xF, true)); }
DI bf16x8 ldsr128(LAS char* l, unsigned off) { return *(LAS bf16x8*)(l + off); }
DI bf16x4 ldstr(LAS char* l, unsigned off) { return __builtin_amdgcn_ds_read_tr16_b64_v4bf16((LAS bf16x4*)(l + off)); }
DI bf16x8 cat(bf16x4 a, bf16x4 b) { return __builtin_shufflevector(a, b, 0, 1, 2, 3, 4, 5, 6, 7); }
#define STAMP(k) do {} while (0)
#define PROF_ARG
#define PROF_PASS
#define PROF_FLUSH() do {} while (0)

DI u32x4 pk8(const f32x16& a, int o) { return (u32x4){pk2(a[o], a[o + 1]), pk2(a[o + 2], a[o + 3]), pk2(a[o + 4], a[o + 5]), pk2(a[o + 6], a[o + 7])}; }
DI void scan_step(float& o0, float& o1, float& o2, float& o3, float& o4, float u0, float u1, float u2, float u3, float u4,
                  const float (&s)[5], const float (&a)[5], const float (&b)[5]) {
  asm("v_fma_f32 %0, %15, %10, %5\n\tv_fma_f32 %1, %16, %11, %6\n\tv_fma_f32 %2, %17, %12, %7\n\tv_fma_f32 %3, %18, %13, %8\n\tv_fma_f32 %4, %19, %14, %9\n\t"
      "v_fmac_f32_dpp %0, %10, %20 quad_perm:[1,0,3,2] row_mask:0xf bank_mask:0xf\n\t"
      "v_fmac_f32_dpp %1, %11, %21 quad_perm:[1,0,3,2] row_mask:0xf bank_mask:0xf\n\t"
      "v_fmac_f32_dpp %2, %12, %22 quad_perm:[1,0,3,2] row_mask:0xf bank_mask:0xf\n\t"
      "v_fmac_f32_dpp %3, %13, %23 quad_perm:[1,0,3,2] row_mask:0xf bank_mask:0xf\n\t"
      "v_fmac_f32_dpp %4, %14, %24 quad_perm:[1,0,3,2] row_mask:0xf bank_mask:0xf"
      : "=&v"(o0), "=&v"(o1), "=&v"(o2), "=&v"(o3), "=&v"(o4)
      : "v"(u0), "v"(u1), "v"(u2), "v"(u3), "v"(u4), "v"(s[0]), "v"(s[1]), "v"(s[2]), "v"(s[3]), "v"(s[4]),
        "v"(a[0]), "v"(a[1]), "v"(a[2]), "v"(a[3]), "v"(a[4]), "v"(b[0]), "v"(b[1]), "v"(b[2]), "v"(b[3]), "v"(b[4]));
}

DI void scan_step_ip(float& u0, float& u1, float& u2, float& u3, float& u4, const float (&s)[5], const float (&a)[5], const float (&b)[5]) {
  asm("v_fmac_f32 %0, %10, %5\n\tv_fmac_f32 %1, %11, %6\n\tv_fmac_f32 %2, %12, %7\n\tv_fmac_f32 %3, %13, %8\n\tv_fmac_f32 %4, %14, %9\n\t"
      "v_fmac_f32_dpp %0, %5, %15 quad_perm:[1,0,3,2] row_mask:0xf bank_mask:0xf\n\t"
      "v_fmac_f32_dpp %1, %6, %16 quad_perm:[1,0,3,2] row_mask:0xf bank_mask:0xf\n\t"
      "v_fmac_f32_dpp %2, %7, %17 quad_perm:[1,0,3,2] row_mask:0xf bank_mask:0xf\n\t"
      "v_fmac_f32_dpp %3, %8, %18 quad_perm:[1,0,3,2] row_mask:0xf bank_mask:0xf\n\t"
      "v_fmac_f32_dpp %4, %9, %19 quad_perm:[1,0,3,2] row_mask:0xf bank_mask:0xf"
      : "+v"(u0), "+v"(u1), "+v"(u2), "+v"(u3), "+v"(u4)
      : "v"(s[0]), "v"(s[1]), "v"(s[2]), "v"(s[3]), "v"(s[4]),
        "v"(a[0]), "v"(a[1]), "v"(a[2]), "v"(a[3]), "v"(a[4]), "v"(b[0]), "v"(b[1]), "v"(b[2]), "v"(b[3]), "v"(b[4]));
}
DI void poll_flags(const char* base, unsigned stride, int n, int lane, u64 tag) {
  const GAS u64* fp = (const GAS u64*)(base + (unsigned)(lane < n ? lane : 0) * stride);
  for (unsigned sp = 0; sp < SPIN_CAP; ++sp) {
    const u64 v = __hip_atomic_load(fp, __ATOMIC_RELAXED, __HIP_MEMORY_SCOPE_AGENT);
    if (__all(v == tag)) break;
    __builtin_amdgcn_s_sleep(1);
  }
  asm volatile("" ::: "memory");
}

constexpr int RING = 36928;
struct Stage {
  u32x4 sX, sW;
  unsigned vx[4], lx[4], vw[5], lw[5], sx0, sw0, kmul, kc;
};
constexpr int pord(int n) { return n == 0 ? 0 : n == 1 ? 4 : n == 2 ? 5 : n == 3 ? 1 : n == 4 ? 6 : n == 5 ? 2 : n == 6 ? 7 : n == 7 ? 3 : 8; }
template <int KC, int N, int KMUL>
DI void g1_issue(Stage& S) {
  constexpr int u = pord(N), sl = (KC % 3) * RING;
  if constexpr (u < 4) dma16s<sl>(S.sX, S.vx[u & 3], S.sx0 + S.kc, S.lx[u & 3]);
  else dma16s<sl + 16384>(S.sW, S.vw[u >= 4 ? u - 4 : 0], S.sw0 + S.kc * (KMUL ? KMUL : S.kmul), S.lw[u >= 4 ? u - 4 : 0]);
  if (N == 8) kadv(S.kc);
}
template <int KC, int KMUL>
DI void g1_all(Stage& S) {
  g1_issue<KC, 0, KMUL>(S); g1_issue<KC, 1, KMUL>(S); g1_issue<KC, 2, KMUL>(S); g1_issue<KC, 3, KMUL>(S); g1_issue<KC, 4, KMUL>(S);
  g1_issue<KC, 5, KMUL>(S); g1_issue<KC, 6, KMUL>(S); g1_issue<KC, 7, KMUL>(S); g1_issue<KC, 8, KMUL>(S);
}
#define LABEL(L) asm volatile(L ":" ::: "memory")
#define INONE(n) do {} while (0)
#define NOP12(...) asm volatile("s_nop 7\n\ts_nop 3" : __VA_ARGS__)
constexpr u64 TAG_STEP = 0x9E3779B97F4A7C15ull;

constexpr int TB = 3 * RING, T_PAR = TB, T_POW = TB + 2560, T_E = TB + 7680, T_L = 102400, T_H = 107520;
constexpr int cbase(int ci, int part) {
  return (ci == 3 || ci == 4 ? (ci - 3) * 20480 : (ci == 5 || ci == 6) ? TB + (ci - 5) * 20480 : 40960 + (ci >= 7 ? ci - 7 : ci) * 20480) + part * 10240;
}
constexpr int late_dma(int ks, int slot) {
  int n = 0;
  for (int k = 11; k <= 21; ++k)
    for (int sl = 0; sl < 2; ++sl) {
      const bool on = sl == 0 || k == 12 || k == 14 || k == 16 || k == 18;
      if (k == ks && sl == slot) return on ? n : -1;
      if (on) ++n;
    }
  return -1;
}
struct Disc { float ld, a, b, dt, e, y, n, r, z, sp, cp, lr, li, cr, ci, t0, t1, t2, qr, qi, pr, pi; };
#define PIN1(x) asm volatile("" : "+v"(x))
#define PIN2(x, y) asm volatile("" : "+v"(x), "+v"(y))
#define PIN3(x, y, z) asm volatile("" : "+v"(x), "+v"(y), "+v"(z))
template <int K>
DI void disc_slice(Disc& D, LAS char* lds, int pl) {
  constexpr float L2E = 1.4426950408889634f;
  if constexpr (K == 0) { D.dt = rbf(__builtin_amdgcn_exp2f(D.ld * L2E)); PIN1(D.dt); }
  if constexpr (K == 1) { D.e = __builtin_amdgcn_exp2f((D.dt * D.a) * L2E); D.y = D.dt * D.b; PIN2(D.e, D.y); }
  if constexpr (K == 2) { D.n = __builtin_rintf(D.y * 0.6366197723675814f); D.r = __builtin_fmaf(D.n, -1.5703125f, D.y); PIN2(D.n, D.r); }
  if constexpr (K == 3) { D.r = __builtin_fmaf(D.n, -4.837512969970703125e-4f, D.r); D.r = __builtin_fmaf(D.n, -7.54978995489188216e-8f, D.r); D.z = D.r * D.r; PIN2(D.r, D.z); }
  if constexpr (K == 4) { D.sp = __builtin_fmaf(__builtin_fmaf(D.z, -1.9515295891e-4f, 8.3321608736e-3f), D.z, -1.6666654611e-1f); D.t0 = D.r * D.z; PIN2(D.sp, D.t0); }
  if constexpr (K == 5) { D.sp = __builtin_fmaf(D.sp, D.t0, D.r); D.cp = __builtin_fmaf(__builtin_fmaf(D.z, 2.443315711809948e-5f, -1.388731625493765e-3f), D.z, 4.166664568298827e-2f); PIN2(D.sp, D.cp); }
  if constexpr (K == 6) { D.t0 = D.z * D.z; D.t1 = __builtin_fmaf(D.z, -0.5f, 1.f); D.cp = __builtin_fmaf(D.cp, D.t0, D.t1); PIN1(D.cp); }
  if constexpr (K == 7) { const int q = (int)D.n; D.t2 = __int_as_float(q); D.t0 = (q & 1) ? D.cp : D.sp; D.t1 = (q & 1) ? D.sp : D.cp; PIN3(D.t0, D.t1, D.t2); }
  if constexpr (K == 8) { const unsigned q = __float_as_uint(D.t2); D.t0 = __uint_as_float(__float_as_uint(D.t0) ^ ((q & 2u) << 30)); D.t2 = __uint_as_float(((q + 1u) & 2u) << 30); PIN2(D.t0, D.t2); }
  if constexpr (K == 9) { D.t1 = __uint_as_float(__float_as_uint(D.t1) ^ __float_as_uint(D.t2)); D.li = D.e * D.t0; D.lr = D.e * D.t1; PIN2(D.lr, D.li); }
  if constexpr (K == 10) { D.t0 = __builtin_amdgcn_rcpf(fmaxf(fabsf(D.a), fabsf(D.b))); D.t2 = D.lr - 1.f; PIN2(D.t0, D.t2); }
  if constexpr (K == 11) { D.a *= D.t0; D.b *= D.t0; D.t1 = D.a * D.a; PIN3(D.a, D.b, D.t1); }
  if constexpr (K == 12) { D.t1 = __builtin_amdgcn_rcpf(__builtin_fmaf(D.b, D.b, D.t1)); D.cr = D.t2 * D.a; PIN2(D.t1, D.cr); }
  if constexpr (K == 13) { D.t1 *= D.t0; D.cr = __builtin_fmaf(D.li, D.b, D.cr); D.ci = D.li * D.a; PIN3(D.t1, D.cr, D.ci); }
  if constexpr (K == 14) { D.ci = __builtin_fmaf(-D.t2, D.b, D.ci) * D.t1; D.cr *= D.t1; PIN2(D.cr, D.ci); }
  if constexpr (K == 15) { *(LAS f32x4*)(lds + T_PAR + pl * 32) = (f32x4){D.lr, D.li, D.cr, D.ci}; D.qr = D.lr; D.qi = D.li; }
  if constexpr (K >= 16 && K < 20) { const float t = __builtin_fmaf(D.qr, D.qr, -D.qi * D.qi); D.qi = (D.qr + D.qr) * D.qi; D.qr = t; PIN2(D.qr, D.qi); }
  if constexpr (K == 20) { *(LAS f32x2*)(lds + T_POW + pl * 8) = (f32x2){1.f, 0.f}; D.pr = D.qr; D.pi = D.qi; }
  if constexpr (K >= 21 && K < 28) {
    *(LAS f32x2*)(lds + T_POW + ((K - 20) * 80 + pl) * 8) = (f32x2){D.pr, D.pi};
    const float t = __builtin_fmaf(D.pr, D.qr, -D.pi * D.qi); D.pi = __builtin_fmaf(D.pr, D.qi, D.pi * D.qr); D.pr = t; PIN2(D.pr, D.pi);
  }
  if constexpr (K == 28) *(LAS f32x4*)(lds + T_PAR + pl * 32 + 16) = (f32x4){D.pr, D.pi, D.qr, D.qi};
}

DI void chain_b(const Params& P, LAS char* lds, Stage& S, int c, int j, int rot, int sx, int sw, int w, int lane PROF_ARG) {
  LABEL(".Lcb_beg");
  const int g = lane >> 4, i = lane & 15, h = lane >> 5, i32 = lane & 31, e3 = lane >> 3, pos = lane & 7, wr = w >> 1, wc = w & 1;
#define B1ISSUE(KC, N) g1_issue<KC, N, 1>(S)
  const int lg3 = lane < 20 ? 0 : (lane < 40 ? 1 : 2), plc = 20 * w + (lane < 60 ? lane - 20 * lg3 : 0);
  unsigned in0, in1, in2;
  {
    const unsigned po = (j * 80 + plc) * 2;
    asm volatile("global_load_ushort %0, %3, %4\n\tglobal_load_ushort %1, %3, %5\n\tglobal_load_ushort %2, %3, %6"
                 : "=&v"(in0), "=&v"(in1), "=&v"(in2) : "v"(po), "s"(P.logdt), "s"(P.lre), "s"(P.lim) : "memory");
  }
  u64 tag = *(const CAS u64*)(P.ws + WS_EPOCH_B);
  f32x16 acc[5];
  const unsigned ra = (w * 32 + i32) * 128, rb = 16384 + ((i32 & 1) * 80 + (i32 >> 1)) * 128;
  const unsigned ma = (i32 >> 1) & 7, mb = (((i32 >> 2) & 3) << 1) | (i32 & 1);
  LBase<4, 2> aA, aB;
#pragma unroll
  for (int s = 0; s < 4; ++s) { aA.v[0][s] = ra + (((2 * s + h) ^ ma) << 4); aB.v[0][s] = rb + (((2 * s + h) ^ mb) << 4); }
  aA.fin(); aB.fin();
  bf16x8 fa[2][2], fb[2][2][5];
#define B1RD(KC, HF, Q) do { constexpr int sl = ((KC) % 3) * RING, s2 = (Q) / 6, q6 = (Q) % 6; \
    if (q6 == 0) fa[HF][s2] = ldsr128(lds, aA.at<sl>(2 * (HF) + s2)); else fb[HF][s2][q6 - 1] = ldsr128(lds, aB.at<sl + (q6 > 0 ? q6 - 1 : 0) * 2048>(2 * (HF) + s2)); } while (0)
  SCHED();
  g1_all<1, 1>(S);
  STAMP(2);
  asm volatile("s_waitcnt vmcnt(9)" : "+v"(in0), "+v"(in1), "+v"(in2)::"memory");
  bar();
  STAMP(3);
  asm volatile("" : "+s"(tag));
  tag += TAG_STEP;
  const unsigned tagl = (unsigned)tag, tagh = (unsigned)(tag >> 32);
  Disc D; D.ld = bf2f(in0); D.a = bf2f(in1); D.b = bf2f(in2);
#define B1RD2(KC, HF, X) do { B1RD(KC, HF, 2 * (X)); B1RD(KC, HF, 2 * (X) + 1); } while (0)
  B1RD2(0, 0, 0); B1RD2(0, 0, 1); B1RD2(0, 0, 2); B1RD2(0, 0, 3); B1RD2(0, 0, 4); B1RD2(0, 0, 5);
  SCHED();
  const int part = lane & 1, sub = 2 * w + h, pl0 = i32 >> 1;
  float la[5], lb[5], ko[5], kx[5], k1[5], k2[5];
  f32x4 tpp; f32x2 tpw;
  const int ue = (wr + sx) & 3, uo = (2 + wr + sx) & 3;
  const u32x4 sCe = mksrd((ue & 1) ? P.cim : P.cre, 640 * 1280), sCo = mksrd((uo & 1) ? P.cim : P.cre, 640 * 1280);
  unsigned vC[5];
  unsigned pb[5];
#pragma unroll
  for (int k = 0; k < 5; ++k) pb[k] = k + (rot >> 1) >= 5 ? k + (rot >> 1) - 5 : k + (rot >> 1);
#define B2ISSUE(CI, U) dma16s<cbase(CI, 0)>(((CI) & 1) ? sCo : sCe, vC[U], pb[(CI) >> 1] * 256 + ((((CI) & 1) ? uo : ue) >> 1) * 128, S.lw[U])
#define BCONST(K) do { constexpr int n_ = (K) / 4, ph_ = (K) % 4; \
    if constexpr ((K) < 20) { \
      if constexpr (ph_ == 0) { tpp = *(LAS f32x4*)(lds + T_PAR + (pl0 + 16 * n_) * 32); tpw = *(LAS f32x2*)(lds + T_POW + (sub * 80 + pl0 + 16 * n_) * 8); } \
      if constexpr (ph_ == 1) { la[n_] = tpp[0]; lb[n_] = part ? tpp[1] : -tpp[1]; kx[n_] = -2.f * tpp[3]; tpp[2] *= 2.f; ko[n_] = part ? -tpp[2] : tpp[2]; PIN3(lb[n_], kx[n_], ko[n_]); } \
      if constexpr (ph_ == 2) { k1[n_] = __builtin_fmaf(tpp[2], tpw[0], kx[n_] * tpw[1]); k2[n_] = __builtin_fmaf(-tpp[2], tpw[1], kx[n_] * tpw[0]); PIN2(k1[n_], k2[n_]); } \
      if constexpr (ph_ == 3) { const float t_ = k1[n_]; k1[n_] = part ? k2[n_] : t_; k2[n_] = part ? -t_ : k2[n_]; PIN2(k1[n_], k2[n_]); } \
    } else if constexpr ((K) < 25) {   \
      constexpr int u_ = (K) >= 20 ? (K) - 20 : 0; const int U_ = u_ + sw >= 5 ? u_ + sw - 5 : u_ + sw; \
      const int R_ = 8 * (5 * wc + U_) + e3, d_ = R_ < 64 ? 2 * (R_ & 31) + (R_ >> 5) : R_; \
      vC[u_] = (j * 80 + d_) * 1280 + ((pos ^ ((R_ >> 1) & 7)) << 4); PIN1(vC[u_]); } } while (0)
#define B1GAP(KC, HF, X, RKC, RD, ISS) do { \
    if ((KC) == 0 && (HF) == 0 && (X) < 5) mfma32z(acc[(X) % 5], fa[HF][(X) / 5], fb[HF][(X) / 5][(X) % 5]); \
    else mfma32(acc[(X) % 5], fa[HF][(X) / 5], fb[HF][(X) / 5][(X) % 5]); \
    if (RD) { if ((X) == 0) B1RD2(RKC, 1 - (HF), 0); if ((X) == 1) B1RD2(RKC, 1 - (HF), 1); if ((X) == 3) B1RD2(RKC, 1 - (HF), 2); \
              if ((X) == 4) B1RD2(RKC, 1 - (HF), 3); if ((X) == 6) B1RD2(RKC, 1 - (HF), 4); if ((X) == 7) B1RD2(RKC, 1 - (HF), 5); } \
    if ((KC) == 0 && (HF) == 0) { if ((X) >= 1) B1ISSUE(2, ((X) >= 1 ? (X) - 1 : 0)); } \
    else if ((KC) == 9 && (HF) == 1) { B2ISSUE(3 + (X) / 5, (X) % 5); } \
    else { if ((X) == 2) { ISS(0); } if ((X) == 4) { ISS(1); } if ((X) == 5) { ISS(2); } if ((X) == 8) { ISS(3); } if ((X) == 9) { ISS(4); } } \
    { constexpr int sl_ = (HF) * 6 + ((X) == 0 ? 0 : (X) == 1 ? 1 : (X) == 3 ? 2 : (X) == 4 ? 3 : (X) == 6 ? 4 : (X) == 7 ? 5 : -100); \
      if constexpr ((KC) >= 1 && (KC) <= 3 && sl_ >= 0 && ((KC) - 1) * 12 + sl_ <= 28) disc_slice<(sl_ >= 0 ? ((KC) - 1) * 12 + sl_ : 0)>(D, lds, plc); \
      if constexpr ((KC) >= 4 && (KC) <= 6 && sl_ >= 0 && ((KC) - 4) * 12 + sl_ >= 6) BCONST((sl_ >= 0 && (KC) >= 4 ? ((KC) - 4) * 12 + sl_ - 6 : 99)); } \
    SCHED(); } while (0)
#define B1HALF(KC, HF, RKC, RD, ISS) do { B1GAP(KC, HF, 0, RKC, RD, ISS); B1GAP(KC, HF, 1, RKC, RD, ISS); B1GAP(KC, HF, 2, RKC, RD, ISS); B1GAP(KC, HF, 3, RKC, RD, ISS); \
    B1GAP(KC, HF, 4, RKC, RD, ISS); B1GAP(KC, HF, 5, RKC, RD, ISS); B1GAP(KC, HF, 6, RKC, RD, ISS); B1GAP(KC, HF, 7, RKC, RD, ISS); B1GAP(KC, HF, 8, RKC, RD, ISS); B1GAP(KC, HF, 9, RKC, RD, ISS); } while (0)
#define B1STEP(KC, ISSA, VM, ISSB) do { B1HALF(KC, 0, KC, true, ISSA); if ((KC) < 9) vmwait<VM>(); bar(); B1HALF(KC, 1, ((KC) < 9 ? (KC) + 1 : 0), ((KC) < 9), ISSB); } while (0)
#define IA(KC, n) do { if constexpr ((n) >= 1) B1ISSUE((KC) + 2, 4 + (n)); } while (0)
#define IB(KC, n) B1ISSUE((KC) + 3, n)
#define IA1(n) IA(1, n)
#define IA2(n) IA(2, n)
#define IA3(n) IA(3, n)
#define IA4(n) IA(4, n)
#define IA5(n) IA(5, n)
#define IA6(n) IA(6, n)
#define IA7(n) IA(7, n)
#define IB0(n) IB(0, n)
#define IB1(n) IB(1, n)
#define IB2(n) IB(2, n)
#define IB3(n) IB(3, n)
#define IB4(n) IB(4, n)
#define IB5(n) IB(5, n)
#define IB6(n) IB(6, n)
#define IC0(n) B2ISSUE(0, n)
#define IC1(n) B2ISSUE(1, n)
#define IC2(n) B2ISSUE(2, n)
  B1STEP(0, INONE, 9, IB0); B1STEP(1, IA1, 9, IB1); B1STEP(2, IA2, 9, IB2); B1STEP(3, IA3, 9, IB3); B1STEP(4, IA4, 9, IB4);
  B1STEP(5, IA5, 9, IB5); B1STEP(6, IA6, 9, IB6); B1STEP(7, IA7, 9, IC0); B1STEP(8, INONE, 5, IC1); B1STEP(9, IC2, 0, INONE);
  STAMP(4);
  const f32x4 pq = *(LAS f32x4*)(lds + T_PAR + plc * 32 + 16);
  asm volatile("s_nop 7" : "+v"(acc[0]), "+v"(acc[1]), "+v"(acc[2]), "+v"(acc[3]), "+v"(acc[4]));
  {
    float s[5] = {acc[0][0], acc[1][0], acc[2][0], acc[3][0], acc[4][0]};
    asm volatile("s_nop 3" : "+v"(s[0]), "+v"(s[1]), "+v"(s[2]), "+v"(s[3]), "+v"(s[4]));
#pragma unroll
    for (int t = 1; t < 16; ++t) {
      float o[5];
      scan_step(o[0], o[1], o[2], o[3], o[4], acc[0][t], acc[1][t], acc[2][t], acc[3][t], acc[4][t], s, la, lb);
#pragma unroll
      for (int n = 0; n < 5; ++n) s[n] = o[n];
    }
    const unsigned ea = T_E + (sub * 160 + i32) * 4;
#pragma unroll
    for (int n = 0; n < 5; ++n) *(LAS float*)(lds + ea + n * 128) = s[n];
  }
  bar();
  STAMP(5);
  const __amdgpu_buffer_rsrc_t rG = mkrs(P.ws + WS_GB1 + j * 20480, 20480);
  {
    float hr = 0.f, hi = 0.f;
    f32x2 e[8];
    const unsigned eb = T_E + plc * 8, lb_ = T_L + plc * 8;
#pragma unroll
    for (int k = 0; k < 8; ++k) e[k] = *(LAS f32x2*)(lds + eb + k * 640);
    f32x2 lp[8];
#pragma unroll
    for (int k = 0; k < 8; ++k) {
      lp[k] = (f32x2){hr, hi};
      const float t = __builtin_fmaf(pq[2], hr, __builtin_fmaf(-pq[3], hi, e[k][0])); hi = __builtin_fmaf(pq[2], hi, __builtin_fmaf(pq[3], hr, e[k][1])); hr = t;
    }
    const u32x4 gr = {__float_as_uint(hr), tagl, __float_as_uint(hi), tagh};
    __builtin_amdgcn_raw_buffer_store_b128(gr, rG, lane < 20 ? plc * 16 : 0x7fffff00, c * 1280, 16);
#pragma unroll
    for (int k = 0; k < 8; ++k) *(LAS f32x2*)(lds + lb_ + k * 640) = lp[k];
  }
  STAMP(6);
  unsigned trw = TB + w * 12800 + i32 * 80 + h * 32, trr = TB + w * 12800 + (16 * h + 2 * (i >> 2)) * 80 + (g & 1) * 32 + (i & 3) * 8;
  unsigned la_ = T_L + (sub * 80 + pl0) * 8, ha_ = T_H + pl0 * 8, hw_ = T_H + lg3 * 640 + plc * 8;
  asm volatile("" : "+v"(trw), "+v"(trr), "+v"(la_), "+v"(ha_), "+v"(hw_));
  float w5r, w5i;
  {
    const float p2r = pq[0] * pq[0] - pq[1] * pq[1], p2i = 2.f * pq[0] * pq[1], p4r = p2r * p2r - p2i * p2i, p4i = 2.f * p2r * p2i;
    const float p5r = p4r * pq[0] - p4i * pq[1], p5i = p4r * pq[1] + p4i * pq[0];
    w5r = lg3 == 0 ? 1.f : (lg3 == 1 ? p5r : p5r * p5r - p5i * p5i); w5i = lg3 == 0 ? 0.f : (lg3 == 1 ? p5i : 2.f * p5r * p5i);
    PIN2(w5r, w5i);
  }
#define BSCALE(X) do { constexpr int n = (X) >> 2, q = ((X) & 3) * 4; \
    float u0 = acc[n][q], u1 = acc[n][q + 1], u2 = acc[n][q + 2], u3 = acc[n][q + 3], t0, t1, t2, t3; \
    asm volatile("s_nop 1\n\tv_mul_f32_dpp %4, %0, %8 quad_perm:[1,0,3,2] row_mask:0xf bank_mask:0xf\n\tv_mul_f32_dpp %5, %1, %8 quad_perm:[1,0,3,2] row_mask:0xf bank_mask:0xf\n\t" \
                 "v_mul_f32_dpp %6, %2, %8 quad_perm:[1,0,3,2] row_mask:0xf bank_mask:0xf\n\tv_mul_f32_dpp %7, %3, %8 quad_perm:[1,0,3,2] row_mask:0xf bank_mask:0xf\n\t" \
                 "v_fma_f32 %0, %0, %9, %4\n\tv_fma_f32 %1, %1, %9, %5\n\tv_fma_f32 %2, %2, %9, %6\n\tv_fma_f32 %3, %3, %9, %7" \
                 : "+v"(u0), "+v"(u1), "+v"(u2), "+v"(u3), "=&v"(t0), "=&v"(t1), "=&v"(t2), "=&v"(t3) : "v"(kx[n]), "v"(ko[n])); \
    acc[n][q] = u0; acc[n][q + 1] = u1; acc[n][q + 2] = u2; acc[n][q + 3] = u3; } while (0)
#define BSCALE5(X) do { BSCALE(X); BSCALE(X + 1); BSCALE(X + 2); BSCALE(X + 3); BSCALE(X + 4); } while (0)
  BSCALE5(0); BSCALE5(5); BSCALE5(10); BSCALE5(15);
  STAMP(7);
  {
    u32x4 gv[5];
    const int cl = c - 5 - 5 * lg3;
    const int vo = cl * 1280 + plc * 16;
    for (unsigned sp = 0; sp < SPIN_CAP; ++sp) {
      bool ok = true;
#pragma unroll
      for (int k = 0; k < 5; ++k) gv[k] = __builtin_amdgcn_raw_buffer_load_b128(rG, cl + k >= 0 ? vo + k * 1280 : 0x7fffff00, 0, 16);
#pragma unroll
      for (int k = 0; k < 5; ++k) ok &= (cl + k < 0) | ((gv[k][1] == tagl) & (gv[k][3] == tagh));
      if (__all(ok)) break;
      asm volatile("" ::: "memory");
    }
    STAMP(8);
    float hr = __uint_as_float(gv[0][0]), hi = __uint_as_float(gv[0][2]);
#pragma unroll
    for (int k = 1; k < 5; ++k) {
      const float t = __builtin_fmaf(pq[0], hr, __builtin_fmaf(-pq[1], hi, __uint_as_float(gv[k][0])));
      hi = __builtin_fmaf(pq[0], hi, __builtin_fmaf(pq[1], hr, __uint_as_float(gv[k][2]))); hr = t;
    }
    if (lane < 60) *(LAS f32x2*)(lds + hw_) = (f32x2){w5r * hr - w5i * hi, w5r * hi + w5i * hr};
  }
  bar();
  STAMP(9);
  float s[5];
  {
#pragma unroll
    for (int n = 0; n < 5; ++n) {
      const f32x2 L = *(LAS f32x2*)(lds + la_ + n * 128);
      const f32x2 H = (*(LAS f32x2*)(lds + ha_ + n * 128) + *(LAS f32x2*)(lds + ha_ + 640 + n * 128)) + *(LAS f32x2*)(lds + ha_ + 1280 + n * 128);
      s[n] = __builtin_fmaf(k1[n], H[0], __builtin_fmaf(k2[n], H[1], __builtin_fmaf(ko[n], part ? L[1] : L[0], kx[n] * (part ? L[0] : L[1]))));
      lb[n] = -lb[n];
    }
  }
#pragma unroll
  for (int t = 0; t < 16; ++t) {
    float u0 = acc[0][t], u1 = acc[1][t], u2 = acc[2][t], u3 = acc[3][t], u4 = acc[4][t];
    scan_step_ip(u0, u1, u2, u3, u4, s, la, lb);
    acc[0][t] = s[0] = u0; acc[1][t] = s[1] = u1; acc[2][t] = s[2] = u2; acc[3][t] = s[3] = u3; acc[4][t] = s[4] = u4;
    if (t >= 8 && t < 13) { *(LAS u32x4*)(lds + trw + (t - 8) * 2560) = pk8(acc[t >= 8 && t < 13 ? t - 8 : 0], 0); SCHED(); }
  }
#pragma unroll
  for (int n = 0; n < 5; ++n) *(LAS u32x4*)(lds + trw + n * 2560 + 16) = pk8(acc[n], 8);
  const __amdgpu_buffer_rsrc_t rH = mkrs(P.ws + WS_HB + c * 327680, 327680);
  const unsigned vhl = w * 1024 + lane * 16;
  {
#pragma unroll
    for (int n = 0; n < 5; ++n)
#pragma unroll
      for (int pt = 0; pt < 2; ++pt) {
        const bf16x8 v = cat(ldstr(lds, trr + n * 2560 + pt * 80), ldstr(lds, trr + n * 2560 + pt * 80 + 640));
        __builtin_amdgcn_raw_buffer_store_b128(__builtin_bit_cast(u32x4, v), rH, vhl, (pt * 40 + j * 5 + n) * 4096, 16);
      }
  }
  STAMP(10);
  unsigned xa[16], xb[8], dd2, dd4;
  {
    const u32x4 sB = mksrd(P.Bx, 2048 * 1280);
    const unsigned xo = (c * 128 + 32 * w) * 1280 + j * 160, va = h * 5120 + i32 * 4, vb = g * 5120 + 128 + i * 2;
#define XLD4(R) asm volatile("buffer_load_dword %0, %4, %5, %6 offen\n\tbuffer_load_dword %1, %4, %5, %6 offen offset:1280\n\tbuffer_load_dword %2, %4, %5, %6 offen offset:2560\n\tbuffer_load_dword %3, %4, %5, %6 offen offset:3840" \
      : "=&v"(xa[4 * (R)]), "=&v"(xa[4 * (R) + 1]), "=&v"(xa[4 * (R) + 2]), "=&v"(xa[4 * (R) + 3]) : "v"(va), "s"(sB), "s"(xo + (R) * 10240) : "memory")
#define XLD2(R) asm volatile("buffer_load_ushort %0, %4, %5, %6 offen\n\tbuffer_load_ushort %1, %4, %5, %6 offen offset:1280\n\tbuffer_load_ushort %2, %4, %5, %6 offen offset:2560\n\tbuffer_load_ushort %3, %4, %5, %6 offen offset:3840" \
      : "=&v"(xb[4 * (R)]), "=&v"(xb[4 * (R) + 1]), "=&v"(xb[4 * (R) + 2]), "=&v"(xb[4 * (R) + 3]) : "v"(vb), "s"(sB), "s"(xo + (R) * 20480) : "memory")
    XLD4(0); XLD4(1); XLD4(2); XLD4(3); XLD2(0); XLD2(1);
    const unsigned v2 = (j * 80 + 2 * i32) * 2, v4 = (j * 80 + 64 + i) * 2;
    asm volatile("global_load_dword %0, %2, %4\n\tglobal_load_ushort %1, %3, %4" : "=&v"(dd2), "=&v"(dd4) : "v"(v2), "v"(v4), "s"(P.dsk) : "memory");
  }
  bar();
  B2ISSUE(5, 0); B2ISSUE(5, 1); B2ISSUE(5, 2); B2ISSUE(5, 3); B2ISSUE(5, 4);
  vmwait<31>();
  if (lane == 0) __hip_atomic_store((GAS u64*)(P.ws + WS_FB2 + ((c * 4 + w) * 8 + j) * 128), tag, __ATOMIC_RELAXED, __HIP_MEMORY_SCOPE_AGENT);
  STAMP(11);
  B2ISSUE(6, 0); B2ISSUE(6, 1); B2ISSUE(6, 2); B2ISSUE(6, 3); B2ISSUE(6, 4);
  f32x16 y[2];
  f32x4 z[2];
  {
    asm volatile("s_waitcnt vmcnt(11)" : "+v"(xa[0]), "+v"(xa[1]), "+v"(xa[2]), "+v"(xa[3]), "+v"(xa[4]), "+v"(xa[5]), "+v"(xa[6]), "+v"(xa[7]), "+v"(xa[8]), "+v"(xa[9]),
                 "+v"(xa[10]), "+v"(xa[11]), "+v"(xa[12]), "+v"(xa[13]), "+v"(xa[14]), "+v"(xa[15]), "+v"(dd2), "+v"(dd4));
    asm volatile("" : "+v"(xb[0]), "+v"(xb[1]), "+v"(xb[2]), "+v"(xb[3]), "+v"(xb[4]), "+v"(xb[5]), "+v"(xb[6]), "+v"(xb[7]));
    const float d0 = bf2f(dd2 & 0xffffu), d1 = bf2f(dd2 >> 16), d4 = bf2f(dd4);
#define YINIT(R) do { const unsigned v_ = pk2(bf2f(xa[R] & 0xffffu) * d0, __uint_as_float(xa[R] & 0xffff0000u) * d1); \
      y[0][R] = __uint_as_float(v_ << 16); y[1][R] = __uint_as_float(v_ & 0xffff0000u); } while (0)
    YINIT(0); YINIT(1); YINIT(2); YINIT(3); YINIT(4); YINIT(5); YINIT(6); YINIT(7); YINIT(8); YINIT(9); YINIT(10); YINIT(11); YINIT(12); YINIT(13); YINIT(14); YINIT(15);
#pragma unroll
    for (int r = 0; r < 8; ++r) z[r >> 2][r & 3] = rbf(bf2f(xb[r]) * d4);
    asm volatile("" : "+v"(y[0]), "+v"(y[1]), "+v"(z[0]), "+v"(z[1]));
  }
  LBase<6, 3> aC;
#pragma unroll
  for (int s_ = 0; s_ < 4; ++s_) aC.v[0][s_] = i32 * 128 + (((2 * s_ + h) ^ ma) << 4);
#pragma unroll
  for (int s_ = 0; s_ < 2; ++s_) aC.v[0][4 + s_] = (64 + i) * 128 + (((4 * s_ + 2 * (g & 1) + (g >> 1)) ^ (i >> 1)) << 4);
  aC.fin();
  bf16x8 fc[2][5];
#define B2RD(KS, N) fc[(KS) & 1][N] = ldsr128(lds, aC.at<cbase((KS) >> 2, ((KS) >> 1) & 1) + ((N) < 4 ? ((N) & 1) * 4096 : 0)>((N) < 4 ? 2 * ((KS) & 1) + ((N) >> 1) : 4 + ((KS) & 1)))
  B2RD(0, 0); B2RD(0, 1); B2RD(0, 2); B2RD(0, 3); B2RD(0, 4);
  poll_flags(P.ws + WS_FB2 + (c * 4 + w) * 1024, 128, 8, lane, tag);
  STAMP(12);
#define XPD 6
  constexpr int PD = XPD;
  u32x4 hf_[PD][2];
  unsigned ho[4];
#pragma unroll
  for (int k = 0; k < 4; ++k) ho[k] = (((k + sx) & 1) * 40 + (((k + sx) >> 1) & 1) * 4) * 4096;
#define B2HLD1(KS, Q) do { if constexpr ((KS) < 40) hf_[(KS) % PD][Q] = __builtin_amdgcn_raw_buffer_load_b128(rH, vhl + (((KS) & 1) * 2 + (Q)) * 4096, ho[((KS) >> 1) & 3] + pb[(KS) >> 3] * 32768, 16); } while (0)
  B2HLD1(0, 0); B2HLD1(0, 1); B2HLD1(1, 0); B2HLD1(1, 1); B2HLD1(2, 0); B2HLD1(2, 1); B2HLD1(3, 0); B2HLD1(3, 1); B2HLD1(4, 0); B2HLD1(4, 1);
  if (c == 15 && j == 0 && w == 0 && lane == 0) __hip_atomic_store((GAS u64*)(P.ws + WS_EPOCH_B), tag, __ATOMIC_RELAXED, __HIP_MEMORY_SCOPE_AGENT);
  SCHED();
#define B2LATE(N) do { if constexpr ((N) >= 0) B2ISSUE(7 + ((N) < 0 ? 0 : (N)) / 5, ((N) < 0 ? 0 : (N)) % 5); } while (0)
#define B2RDX(KS, N) do { if constexpr ((KS) < 40) B2RD(KS, N); } while (0)
#define B2STEP(KS) do { \
    if ((KS) == 27) vmwait<10>(); \
    if ((KS) == 11 || (KS) == 27) bar();   \
    u32x4 f0 = hf_[(KS) % PD][0], f1 = hf_[(KS) % PD][1]; \
    mfma32(y[0], __builtin_bit_cast(bf16x8, f0), fc[(KS) & 1][0]); \
    B2RDX((KS) + 1, 0); B2RDX((KS) + 1, 1); B2RDX((KS) + 1, 2); \
    SCHED(); \
    mfma32(y[1], __builtin_bit_cast(bf16x8, f0), fc[(KS) & 1][1]); \
    B2HLD1((KS) + PD - 1, 0); \
    SCHED(); \
    mfma32(y[0], __builtin_bit_cast(bf16x8, f1), fc[(KS) & 1][2]); \
    B2HLD1((KS) + PD - 1, 1); \
    SCHED(); \
    mfma32(y[1], __builtin_bit_cast(bf16x8, f1), fc[(KS) & 1][3]); \
    swap16x4(f0, f1); \
    B2RDX((KS) + 1, 3); B2RDX((KS) + 1, 4); \
    SCHED(); \
    mfma16x2(z[0], z[1], f0, f1, fc[(KS) & 1][4]); \
    B2LATE(late_dma(KS, 0)); B2LATE(late_dma(KS, 1)); \
    SCHED(); } while (0)
#define B2STEP4(K) do { B2STEP(K); B2STEP(K + 1); B2STEP(K + 2); B2STEP(K + 3); } while (0)
  B2STEP4(0); B2STEP4(4); B2STEP4(8); B2STEP4(12); B2STEP4(16); B2STEP4(20); B2STEP4(24); B2STEP4(28); B2STEP4(32); B2STEP4(36);
  STAMP(13);
  NOP12("+v"(y[0]), "+v"(y[1]), "+v"(z[0]), "+v"(z[1]));
  {
    const __amdgpu_buffer_rsrc_t rO = mkrs(P.out, 2048 * 5120);
    const unsigned so = (c * 128 + 32 * w) * 5120 + (640 + j * 80) * 4;
#pragma unroll
    for (int r = 0; r < 16; ++r) {
      const u32x2 v = {__float_as_uint(y[0][r]), __float_as_uint(y[1][r])};
      __builtin_amdgcn_raw_buffer_store_b64(v, rO, h * 20480 + i32 * 8, so + ((r & 3) + 8 * (r >> 2)) * 5120, 16);
    }
#pragma unroll
    for (int r = 0; r < 8; ++r)
      __builtin_amdgcn_raw_buffer_store_b32(__float_as_uint(z[r >> 2][r & 3]), rO, g * 20480 + 256 + i * 4, so + (16 * (r >> 2) + (r & 3)) * 5120, 16);
  }
  LABEL(".Lcb_end");
}

constexpr int A_R1 = 118976, A_WPHI = 151744, A_KVT = 154944, A_ZERO = 161664, A_ONES = 163200;
constexpr int qbase(int qc) { return qc < 4 ? RING + qc * 20512 : 0; }
constexpr int obase(int pr) { return pr == 0 ? A_R1 : 20512 + (pr - 1) * 25600; }
DI void mfma32a(f32x16& c, bf16x8 a, bf16x8 b) { asm volatile("v_mfma_f32_32x32x16_bf16 %0, %1, %2, %0" : "+v"(c) : "v"(a), "a"(b)); }
DI void mfma32az(f32x16& c, bf16x8 a, bf16x8 b) { asm volatile("v_mfma_f32_32x32x16_bf16 %0, %1, %2, 0" : "=&v"(c) : "v"(a), "a"(b)); }
DI void mfma32fa(f32x16& c, bf16x8 a, u32x4 b) { asm volatile("v_mfma_f32_32x32x16_bf16 %0, %1, %2, %0" : "+v"(c) : "a"(a), "v"(b)); }
DI void mfma32faz(f32x16& c, bf16x8 a, u32x4 b) { asm volatile("v_mfma_f32_32x32x16_bf16 %0, %1, %2, 0" : "=&v"(c) : "a"(a), "v"(b)); }
DI void mfma16z(f32x4& c, bf16x8 a, bf16x8 b) { asm volatile("v_mfma_f32_16x16x32_bf16 %0, %1, %2, 0" : "=&v"(c) : "v"(a), "v"(b)); }
DI u32x4 pk44(const f32x16& a, int oa, const f32x16& b, int ob) { return (u32x4){pk2(a[oa], a[oa + 1]), pk2(a[oa + 2], a[oa + 3]), pk2(b[ob], b[ob + 1]), pk2(b[ob + 2], b[ob + 3])}; }
constexpr int fidx(int T, int u) { return T == 0 ? (u == 4 ? 3 : u) : T == 1 ? 3 + u : 8 + u; }
constexpr bool isfirst(int kh, int m0) { return kh == 0 ? m0 < 5 : (m0 < 3 || m0 >= 8); }

DI float max3(float a, float b, float c) { float r; asm("v_max3_f32 %0, %1, %2, %3" : "=v"(r) : "v"(a), "v"(b), "v"(c)); return r; }
DI float min3(float a, float b, float c) { float r; asm("v_min3_f32 %0, %1, %2, %3" : "=v"(r) : "v"(a), "v"(b), "v"(c)); return r; }
DI unsigned pk2v(f32x2 v) { return __builtin_bit_cast(unsigned, __builtin_convertvector(v, bf16x2)); }
#define DUAL_SOFTMAX(p, o, HOOK) do { \
    constexpr float L2E_ = 1.4426950408889634f; \
    float mx_, mn_; \
    asm("v_max3_f32 %0, %2, %3, %4\n\tv_min3_f32 %1, %2, %3, %4\n\tv_max3_f32 %0, %0, %5, %6\n\tv_min3_f32 %1, %1, %5, %6\n\tv_max3_f32 %0, %0, %7, %8\n\tv_min3_f32 %1, %1, %7, %8\n\t" \
        "v_max3_f32 %0, %0, %9, %10\n\tv_min3_f32 %1, %1, %9, %10\n\tv_max3_f32 %0, %0, %11, %12\n\tv_min3_f32 %1, %1, %11, %12\n\tv_max3_f32 %0, %0, %13, %14\n\tv_min3_f32 %1, %1, %13, %14\n\t" \
        "v_max3_f32 %0, %0, %15, %16\n\tv_min3_f32 %1, %1, %15, %16\n\tv_max3_f32 %0, %0, %17, %18\n\tv_min3_f32 %1, %1, %17, %18\n\tv_max3_f32 %0, %0, %19, %20\n\tv_min3_f32 %1, %1, %19, %20\n\t" \
        "v_max_f32 %0, %0, %21\n\tv_min_f32 %1, %1, %21" : "=&v"(mx_), "=&v"(mn_) \
        : "v"(p[0][0]), "v"(p[0][1]), "v"(p[1][0]), "v"(p[1][1]), "v"(p[2][0]), "v"(p[2][1]), "v"(p[3][0]), "v"(p[3][1]), "v"(p[4][0]), "v"(p[4][1]), \
          "v"(p[5][0]), "v"(p[5][1]), "v"(p[6][0]), "v"(p[6][1]), "v"(p[7][0]), "v"(p[7][1]), "v"(p[8][0]), "v"(p[8][1]), "v"(p[9][0]), "v"(p[9][1])); \
    HOOK(0); \
    const f32x2 cx_ = {-mx_ * L2E_, -mx_ * L2E_}, cn_ = {mn_ * L2E_, mn_ * L2E_}, lp_ = {L2E_, L2E_}, ln_ = {-L2E_, -L2E_}; \
    f32x2 e_[20]; \
    _Pragma("unroll") for (int k = 0; k < 10; ++k) { e_[k] = __builtin_elementwise_fma(p[k], lp_, cx_); e_[10 + k] = __builtin_elementwise_fma(p[k], ln_, cn_); } \
    HOOK(1); \
    _Pragma("unroll") for (int k = 0; k < 20; ++k) { e_[k][0] = __builtin_amdgcn_exp2f(e_[k][0]); e_[k][1] = __builtin_amdgcn_exp2f(e_[k][1]); \
      if (k == 2) { HOOK(2); } if (k == 5) { HOOK(3); } if (k == 8) { HOOK(4); } if (k == 11) { HOOK(5); } if (k == 14) { HOOK(6); } if (k == 17) { HOOK(7); } } \
    HOOK(8); \
    const f32x2 sp_ = ((e_[0] + e_[1]) + (e_[2] + e_[3])) + ((e_[4] + e_[5]) + (e_[6] + e_[7])) + (e_[8] + e_[9]); \
    const f32x2 sn_ = ((e_[10] + e_[11]) + (e_[12] + e_[13])) + ((e_[14] + e_[15]) + (e_[16] + e_[17])) + (e_[18] + e_[19]); \
    const float rp_ = __builtin_amdgcn_rcpf(sp_[0] + sp_[1]), rn_ = __builtin_amdgcn_rcpf(sn_[0] + sn_[1]); \
    HOOK(9); \
    _Pragma("unroll") for (int k = 0; k < 10; ++k) { e_[k] *= (f32x2){rp_, rp_}; e_[10 + k] *= (f32x2){rn_, rn_}; } \
    HOOK(10); \
    _Pragma("unroll") for (int u = 0; u < 5; ++u) { o[u] = (u32x4){pk2v(e_[4 * u]), pk2v(e_[4 * u + 1]), pk2v(e_[4 * u + 2]), pk2v(e_[4 * u + 3])}; if (u == 2) { HOOK(11); } } \
    HOOK(12); } while (0)
#define NOHOOK(n) do {} while (0)
#define FEATURE_MFMA(a0, a1, a2, pp, FILL) do { \
    u32x4 kb_[5] = {pk8(a0, 0), pk8(a0, 8), pk8(a1, 0), pk8(a1, 8), pk8(a2, 0)}; \
    asm volatile("s_nop 1" : "+v"(kb_[0]), "+v"(kb_[1]), "+v"(kb_[2]), "+v"(kb_[3]), "+v"(kb_[4])); \
    mfma32faz(pp[0], wf[fidx(0, 0)], kb_[0]); FILL(0); SCHED(); mfma32faz(pp[2], wf[fidx(2, 0)], kb_[0]); FILL(1); SCHED(); \
    mfma32fa(pp[0], wf[fidx(0, 1)], kb_[1]); FILL(2); SCHED(); mfma32fa(pp[2], wf[fidx(2, 1)], kb_[1]); FILL(3); SCHED(); \
    mfma32fa(pp[0], wf[fidx(0, 2)], kb_[2]); FILL(4); SCHED(); mfma32fa(pp[2], wf[fidx(2, 2)], kb_[2]); FILL(5); SCHED(); \
    mfma32fa(pp[0], wf[fidx(0, 4)], kb_[4]); FILL(6); SCHED(); mfma32fa(pp[2], wf[fidx(2, 3)], kb_[3]); FILL(7); SCHED(); \
    mfma32faz(pp[1], wf[fidx(1, 1)], kb_[1]); FILL(8); SCHED(); mfma32fa(pp[2], wf[fidx(2, 4)], kb_[4]); FILL(9); SCHED(); \
    mfma32fa(pp[1], wf[fidx(1, 2)], kb_[2]); FILL(10); SCHED(); mfma32fa(pp[1], wf[fidx(1, 3)], kb_[3]); FILL(11); SCHED(); \
    mfma32fa(pp[1], wf[fidx(1, 4)], kb_[4]); FILL(12); \
    asm volatile("s_nop 7\n\ts_nop 0" : "+v"(pp[0]), "+v"(pp[2])); SCHED(); } while (0)
template <int HD> DI void head_vals(const f32x16 (&pp)[3], f32x2 (&p)[10]) {
#pragma unroll
  for (int k = 0; k < 8; ++k) p[k] = (f32x2){pp[HD][2 * k], pp[HD][2 * k + 1]};
#pragma unroll
  for (int k = 0; k < 2; ++k) p[8 + k] = (f32x2){pp[2][4 * HD + 2 * k], pp[2][4 * HD + 2 * k + 1]};
}
constexpr int qdma(int K) { return K >= 12 && K < 22 ? (K % 2 == 0 ? (K - 12) / 2 : -1) : (K >= 22 && K <= 28 ? K - 17 : -1); }

DI void chain_a(const Params& P, LAS char* lds, Stage& S, int c, int G, int rot, int sx, int sw, int w, int lane PROF_ARG) {
  LABEL(".Lca_beg");
  const int g = lane >> 4, i = lane & 15, h = lane >> 5, i32 = lane & 31, q = i >> 2, p = i & 3;
#define A1ISSUE(KC, N) g1_issue<KC, N, 1920>(S)
  dma16<A_WPHI, 0>(mksrd(P.wphi, 32 * 800), lane * 16, G * 3200 + w * 1024, w * 1024);
  u64 tag = *(const CAS u64*)(P.ws + WS_EPOCH_A);
  *(LAS u32x2*)(lds + A_ZERO + (w * 64 + lane) * 8) = (u32x2){w == 3 && lane == 0 ? 0x3f80u : 0u, 0u};
  const unsigned swq = (w & 1) * 32 * 3840 + G * 160;
  unsigned kq = rot * 128 + (w >> 1) * 128;
  kq = kq == 1280 ? 0 : kq;
#define AQISSUE(QC, U) do { dma16s<qbase(QC)>(S.sW, S.vw[U], swq + kq * 1920, S.lw[U]); if ((U) == 4) { kadv(kq); kadv(kq); } } while (0)
  LBase<4, 2> aX;
#pragma unroll
  for (int s = 0; s < 4; ++s) aX.v[0][s] = (w * 32 + i32) * 128 + (((2 * s + h) ^ ((i32 >> 1) & 7)) << 4);
  aX.fin();
  LBase<6, 2> aT;
  {
    const unsigned x = 2 * (g & 1) + (p >> 1), hh = p & 1, rowb = (8 * (g >> 1) + 2 * q) * 160;
    aT.v[0][0] = rowb + 8 * (x + 12 * hh);
    aT.v[0][1] = rowb + 8 * (4 + x + 12 * hh);
    aT.v[0][2] = rowb + (x < 2 ? 8 * (8 + x + 2 * hh) : 10272 + 8 * (x - 2 + 10 * hh));
    aT.v[0][3] = rowb + 10272 + 8 * (2 + x + 12 * hh);
    aT.v[0][4] = rowb + 10272 + 8 * (hh ? (x < 2 ? 12 + x : 16 + x) : 6 + x);
    aT.v[0][5] = rowb + 8 * (x < 2 ? 8 + x + 2 * hh : 2 + x + 10 * hh);
  }
  aT.fin();
  SCHED();
  g1_all<1, 1920>(S);
  STAMP(2);
  vmwait<9>(); bar();
  STAMP(3);
  asm volatile("" : "+s"(tag));
  tag += TAG_STEP;
  const unsigned tagl = (unsigned)tag, tagh = (unsigned)(tag >> 32);
  bf16x8 xf[40], fw[4][5];
  f32x16 acc[5];
#define A1RDW(KC, S_, T) fw[S_][T] = cat(ldstr(lds, aT.at<((KC) % 3) * RING + 16384 + (S_) * 2560>(T)), ldstr(lds, aT.at<((KC) % 3) * RING + 16384 + (S_) * 2560 + 160>(T)))
#define A1RDX(KC, S_) xf[4 * (KC) + (S_)] = ldsr128(lds, aX.at<((KC) % 3) * RING>(S_))
#define A1RDG(KC, HF, N) do { constexpr int s0_ = 2 * (HF), s1_ = 2 * (HF) + 1; \
    if ((N) == 0) { A1RDX(KC, s0_); A1RDW(KC, s0_, 0); } if ((N) == 1) { A1RDW(KC, s0_, 1); A1RDW(KC, s0_, 2); } if ((N) == 2) { A1RDW(KC, s0_, 3); } \
    if ((N) == 3) { A1RDW(KC, s0_, 4); A1RDX(KC, s1_); } if ((N) == 4) { A1RDW(KC, s1_, 0); } if ((N) == 5) { A1RDW(KC, s1_, 1); A1RDW(KC, s1_, 2); } \
    if ((N) == 6) { A1RDW(KC, s1_, 3); } if ((N) == 7) { A1RDW(KC, s1_, 4); } } while (0)
  A1RDG(0, 0, 0); A1RDG(0, 0, 1); A1RDG(0, 0, 2); A1RDG(0, 0, 3); A1RDG(0, 0, 4); A1RDG(0, 0, 5); A1RDG(0, 0, 6); A1RDG(0, 0, 7);
  SCHED();
  bf16x8 wf[13];
  const bool same = (p & 1) == h;
  const unsigned xo_ = 2 * (g & 1) + (p >> 1);
  const unsigned b01 = A_WPHI + q * 40 + 8 * xo_, b2 = A_WPHI + q * 40 + 32;
#define WFADDR(T, M0) ((same && ((T) == 2 ? (xo_ < 2 && ((xo_ == 0) == (h ? isfirst(1, M0) : isfirst(0, M0)))) : (h ? isfirst(1, M0) == ((T) == 0) : isfirst(0, M0) == ((T) == 0)))) \
                           ? ((T) == 2 ? b2 : b01) + (h ? ((M0) < 8 ? 1920 : 320) : 0) : (unsigned)A_ZERO)
#define WFBUILD(T, U) wf[fidx(T, U)] = cat(ldstr(lds, WFADDR(T, 2 * (U)) + 320 * (U)), ldstr(lds, WFADDR(T, 2 * (U) + 1) + 320 * (U) + 160))
#define WFSLICE(N) do { if ((N) == 0) WFBUILD(0, 0); if ((N) == 1) WFBUILD(0, 1); if ((N) == 2) WFBUILD(0, 2); if ((N) == 3) WFBUILD(0, 4); if ((N) == 4) WFBUILD(1, 1); \
    if ((N) == 5) WFBUILD(1, 2); if ((N) == 6) WFBUILD(1, 3); if ((N) == 7) WFBUILD(1, 4); if ((N) == 8) WFBUILD(2, 0); if ((N) == 9) WFBUILD(2, 1); \
    if ((N) == 10) WFBUILD(2, 2); if ((N) == 11) WFBUILD(2, 3); if ((N) == 12) WFBUILD(2, 4); } while (0)
  const u32x4 sO = mksrd(P.wo, 640 * 1280);
  unsigned vO[6], lo[6];
  const unsigned lod = (w >> 1) * 32;
#define VOSET(U) do { const unsigned r_ = __umulhi(S.vw[U], 1118482u); vO[U] = S.vw[U] - r_ * 2560; lo[U] = S.lw[U] - lod; PIN1(vO[U]); } while (0)
#define AOISSUE(PR, N) do { const unsigned sb_ = (((PR) + sx) & 3) * 204800 + G * 160; \
    if constexpr ((N) < 5) dma16s<obase(PR)>(sO, vO[(N) < 5 ? (N) : 0], sb_ + w * 40960, lo[(N) < 5 ? (N) : 0]); \
    else if constexpr ((N) == 5) dma16s<obase(PR) + 20480>(sO, vO[5], sb_ + 163840, lo[5]); \
    else dma16s<obase(PR) + 20480>(sO, vO[4], sb_ + 163840, lo[4] - w * 5120); } while (0)
  const unsigned trk = 16 * (g >> 1) + 4 * (g & 1) + q;
  LBase<1, 3> aP, aV;
  unsigned v1b, v1m, ps0, ps1, ps2, ps3, rg, rv;
#define A1GAP(KC, HF, X, RKC, RD, ISS) do { constexpr int s_ = 2 * (HF) + (X) / 5; \
    if ((KC) == 0 && (HF) == 0 && (X) < 5) mfma32az(acc[(X) % 5], fw[s_][(X) % 5], xf[4 * (KC) + s_]); \
    else mfma32a(acc[(X) % 5], fw[s_][(X) % 5], xf[4 * (KC) + s_]); \
    if constexpr (RD && (X) < 8) A1RDG(RKC, 1 - (HF), X); \
    if ((KC) == 0 && (HF) == 0) { if ((X) >= 1) A1ISSUE(2, ((X) >= 1 ? (X) - 1 : 0)); } \
    else { if ((X) == 2) { ISS(0); } if ((X) == 4) { ISS(1); } if ((X) == 6) { ISS(2); } if ((X) == 8) { ISS(3); } if ((X) == 9) { ISS(4); } } \
    if constexpr ((KC) >= 1 && (KC) <= 4 && (X) >= 8) WFSLICE(((KC) - 1) * 4 + (HF) * 2 + (X) - 8); \
    if constexpr ((KC) == 5 && (HF) == 0 && (X) < 5) VOSET((X) < 5 ? (X) : 0); \
    if constexpr ((KC) == 6 && (HF) == 0 && (X) == 7) { aP.v[0][0] = trk * 352 + 80 * w + 8 * p; aP.fin(); } \
    if constexpr ((KC) == 6 && (HF) == 0 && (X) == 9) { aV.v[0][0] = 22528 + trk * 160 + 40 * w + 8 * p; aV.fin(); } \
    if constexpr ((KC) == 6 && (HF) == 1 && (X) == 7) { v1b = p == 1 ? (unsigned)A_ONES : aV.v[0][0] + 32; v1m = p == 1 ? 0u : 1u; PIN2(v1b, v1m); } \
    if constexpr ((KC) == 6 && (HF) == 1 && (X) == 9) { ps0 = lane * 16; ps1 = i <= 4 ? 2560 + (g * 5 + i) * 16 : 0x7fffff00; PIN2(ps0, ps1); } \
    if constexpr ((KC) == 7 && (HF) == 0 && (X) == 7) { ps2 = g < 2 ? ps0 : 0x7fffff00; ps3 = g < 2 ? ps1 : 0x7fffff00; PIN2(ps2, ps3); } \
    if constexpr ((KC) == 7 && (HF) == 0 && (X) == 9) { rg = (w < 2 ? 0 : A_R1) + (32 * (w & 1) + i32) * 352 + 160 * h; rv = (w < 2 ? 0 : A_R1) + 22528 + (32 * (w & 1) + i32) * 160; PIN2(rg, rv); } \
    if constexpr ((KC) == 5 && (HF) == 1 && (X) == 7) { vO[5] = w == 0 ? vO[0] : w == 1 ? vO[1] : w == 2 ? vO[2] : vO[3]; \
      lo[5] = (w == 0 ? lo[0] : w == 1 ? lo[1] : w == 2 ? lo[2] : lo[3]) - w * 5120; PIN1(vO[5]); } \
    SCHED(); } while (0)
#define A1HALF(KC, HF, RKC, RD, ISS) do { A1GAP(KC, HF, 0, RKC, RD, ISS); A1GAP(KC, HF, 1, RKC, RD, ISS); A1GAP(KC, HF, 2, RKC, RD, ISS); A1GAP(KC, HF, 3, RKC, RD, ISS); \
    A1GAP(KC, HF, 4, RKC, RD, ISS); A1GAP(KC, HF, 5, RKC, RD, ISS); A1GAP(KC, HF, 6, RKC, RD, ISS); A1GAP(KC, HF, 7, RKC, RD, ISS); A1GAP(KC, HF, 8, RKC, RD, ISS); A1GAP(KC, HF, 9, RKC, RD, ISS); } while (0)
#define A1STEP(KC, ISSA, VM, ISSB) do { A1HALF(KC, 0, KC, true, ISSA); if ((KC) < 9) vmwait<VM>(); bar(); A1HALF(KC, 1, ((KC) < 9 ? (KC) + 1 : 0), ((KC) < 9), ISSB); } while (0)
#define JA(KC, n) do { if constexpr ((n) >= 1) A1ISSUE((KC) + 2, 4 + (n)); } while (0)
#define JB(KC, n) A1ISSUE((KC) + 3, n)
#define JA1(n) JA(1, n)
#define JA2(n) JA(2, n)
#define JA3(n) JA(3, n)
#define JA4(n) JA(4, n)
#define JA5(n) JA(5, n)
#define JA6(n) JA(6, n)
#define JA7(n) JA(7, n)
#define JB0(n) JB(0, n)
#define JB1(n) JB(1, n)
#define JB2(n) JB(2, n)
#define JB3(n) JB(3, n)
#define JB4(n) JB(4, n)
#define JB5(n) JB(5, n)
#define JB6(n) JB(6, n)
#define JQ0(n) AQISSUE(0, n)
#define JQ1(n) AQISSUE(1, n)
#define JQ2(n) AQISSUE(2, n)
#define JQ3(n) AQISSUE(3, n)
  A1STEP(0, INONE, 9, JB0); A1STEP(1, JA1, 9, JB1); A1STEP(2, JA2, 9, JB2); A1STEP(3, JA3, 9, JB3); A1STEP(4, JA4, 9, JB4);
  A1STEP(5, JA5, 9, JB5); A1STEP(6, JA6, 9, JB6); A1STEP(7, JA7, 9, JQ0); A1STEP(8, INONE, 5, JQ1); A1STEP(9, JQ2, 0, JQ3);
  STAMP(4);
  asm volatile("s_nop 7\n\ts_nop 1" : "+v"(acc[0]), "+v"(acc[1]), "+v"(acc[2]), "+v"(acc[3]), "+v"(acc[4]));
  const __amdgpu_buffer_rsrc_t rP = mkrs(P.ws + WS_KVP + ((G * 4 + w) * 16 + c) * 8192, 8192);
  {
    f32x16 pp[3];
#define VFILL(n) do { if ((n) == 0) *(LAS u32x4*)(lds + rv + 80 * h) = pk8(acc[2], 8); if ((n) == 2) *(LAS u32x4*)(lds + rv + 16 + 96 * h) = pk8(acc[3], 0); \
      if ((n) == 4) *(LAS u32x4*)(lds + rv + 32 + 96 * h) = pk8(acc[3], 8); if ((n) == 6) *(LAS u32x4*)(lds + rv + 48 + 48 * h) = pk8(acc[4], 0); \
      if ((n) == 8) *(LAS u32x4*)(lds + rv + 64 + 80 * h) = pk8(acc[4], 8); } while (0)
    FEATURE_MFMA(acc[0], acc[1], acc[2], pp, VFILL);
    f32x2 pv[10];
    u32x4 ph[5];
    head_vals<0>(pp, pv); DUAL_SOFTMAX(pv, ph, NOHOOK);
#pragma unroll
    for (int u = 0; u < 5; ++u) *(LAS u32x4*)(lds + rg + 16 * u) = ph[u];
    SCHED();
    head_vals<1>(pp, pv); DUAL_SOFTMAX(pv, ph, NOHOOK);
#pragma unroll
    for (int u = 0; u < 5; ++u) *(LAS u32x4*)(lds + rg + 80 + 16 * u) = ph[u];
  }
  bar();
  STAMP(5);
  f32x4 kv[3][2];
  {
    bf16x8 a_[4][3], b_[4][2];
#define KVRD(KS) do { constexpr int ro_ = ((KS) >> 1) * A_R1, r0_ = 32 * ((KS) & 1); \
      a_[KS][0] = cat(ldstr(lds, aP.at<ro_ + r0_ * 352>(0)), ldstr(lds, aP.at<ro_ + (r0_ + 8) * 352>(0))); \
      b_[KS][0] = cat(ldstr(lds, aV.at<ro_ + r0_ * 160>(0)), ldstr(lds, aV.at<ro_ + (r0_ + 8) * 160>(0))); \
      b_[KS][1] = cat(ldstr(lds, v1b + v1m * (ro_ + r0_ * 160)), ldstr(lds, v1b + v1m * (ro_ + (r0_ + 8) * 160))); \
      a_[KS][1] = cat(ldstr(lds, aP.at<ro_ + r0_ * 352 + 32>(0)), ldstr(lds, aP.at<ro_ + (r0_ + 8) * 352 + 32>(0))); \
      a_[KS][2] = cat(ldstr(lds, aP.at<ro_ + r0_ * 352 + 64>(0)), ldstr(lds, aP.at<ro_ + (r0_ + 8) * 352 + 64>(0))); } while (0)
#define KVMM(KS) do { _Pragma("unroll") for (int m = 0; m < 3; ++m) _Pragma("unroll") for (int n = 0; n < 2; ++n) { \
      if ((KS) == 0) mfma16z(kv[m][n], a_[KS][m], b_[KS][n]); else mfma16(kv[m][n], a_[KS][m], b_[KS][n]); } } while (0)
    KVRD(0); KVRD(1); SCHED(); KVMM(0); KVRD(2); SCHED(); KVMM(1); KVRD(3); SCHED(); KVMM(2); SCHED(); KVMM(3);
    asm volatile("s_nop 7" : "+v"(kv[0][0]), "+v"(kv[0][1]), "+v"(kv[1][0]), "+v"(kv[1][1]), "+v"(kv[2][0]), "+v"(kv[2][1]));
    vmwait<0>(); bar();
  }
#define KVST(N) do { constexpr int m_ = (N) / 4, n_ = ((N) >> 1) & 1, pl_ = (N) & 1; \
    __builtin_amdgcn_raw_buffer_store_b128((u32x4){__float_as_uint(kv[m_][n_][2 * pl_]), tagl, __float_as_uint(kv[m_][n_][2 * pl_ + 1]), tagh}, rP, \
                                           m_ < 2 ? (n_ ? ps1 : ps0) : (n_ ? ps3 : ps2), pl_ * 4096 + m_ * (n_ ? 320 : 1024), 16); } while (0)
  STAMP(6);
  f32x16 aq[3];
  bf16x8 fq[3][3];
  u32x4 hp[8];
  const u32x4 sP = mksrd(P.ws + WS_KVP + (G * 4 + w) * 131072, 131072);
  const unsigned vp = g * 32768 + i * 16;
#define HPLOAD() asm volatile("s_nop 4\n\tbuffer_load_dwordx4 %0, %8, %9, %10 offen sc1\n\tbuffer_load_dwordx4 %1, %8, %9, %11 offen sc1\n\t" \
      "buffer_load_dwordx4 %2, %8, %9, %12 offen sc1\n\tbuffer_load_dwordx4 %3, %8, %9, %13 offen sc1\n\t" \
      "buffer_load_dwordx4 %4, %8, %9, %14 offen sc1\n\tbuffer_load_dwordx4 %5, %8, %9, %15 offen sc1\n\t" \
      "buffer_load_dwordx4 %6, %8, %9, %16 offen sc1\n\tbuffer_load_dwordx4 %7, %8, %9, %17 offen sc1" \
      : "=&v"(hp[0]), "=&v"(hp[1]), "=&v"(hp[2]), "=&v"(hp[3]), "=&v"(hp[4]), "=&v"(hp[5]), "=&v"(hp[6]), "=&v"(hp[7]) \
      : "v"(vp), "s"(sP), "s"(c * 256), "s"(c * 256 + 4096), "s"(c * 256 + 8192), "s"(c * 256 + 12288), "s"(c * 256 + 16384), "s"(c * 256 + 20480), \
        "s"(c * 256 + 24576), "s"(c * 256 + 28672) : "memory")
#define HPWAIT() asm volatile("s_waitcnt vmcnt(0)" : "+v"(hp[0]), "+v"(hp[1]), "+v"(hp[2]), "+v"(hp[3]), "+v"(hp[4]), "+v"(hp[5]), "+v"(hp[6]), "+v"(hp[7])::"memory")
#define AQOFF(K) (qbase((K) >> 3) + (((K) >> 2) & 1) * 10272 + ((K) & 3) * 2560)
#define AQRD(K, T) fq[(K) % 3][T] = cat(ldstr(lds, aT.at<AQOFF(K)>((T) == 2 ? 5 : (T))), ldstr(lds, aT.at<AQOFF(K) + 160>((T) == 2 ? 5 : (T))))
  AQRD(0, 0); AQRD(0, 1); AQRD(0, 2); AQRD(1, 0); AQRD(1, 1); AQRD(1, 2);
  SCHED();
#define AQGAP(K, T) do { if ((K) == 0) mfma32az(aq[T], fq[(K) % 3][T], xf[K]); else mfma32a(aq[T], fq[(K) % 3][T], xf[K]); \
    if constexpr ((K) + 2 < 40) AQRD((K) + 2, T); \
    if constexpr ((K) < 12 && (T) == 1) KVST((K) < 12 ? (K) : 0); \
    if constexpr ((T) == 0) { constexpr int d_ = qdma(K); if constexpr (d_ >= 0 && d_ < 5) AQISSUE(4, (d_ >= 0 ? d_ : 0) % 5); \
      if constexpr (d_ >= 5) AOISSUE(0, (d_ >= 5 ? d_ - 5 : 0)); } \
    SCHED(); } while (0)
#define AQSTEP(K) do { if ((K) == 6) STAMP(17); if ((K) == 24) STAMP(18); if ((K) == 30) { STAMP(19); vmwait<7>(); bar(); STAMP(20); }   \
    if ((K) == 31) { HPLOAD(); SCHED(); } if ((K) == 32) STAMP(21); \
    AQGAP(K, 0); AQGAP(K, 1); AQGAP(K, 2); } while (0)
#define AQSTEP5(K) do { AQSTEP(K); AQSTEP(K + 1); AQSTEP(K + 2); AQSTEP(K + 3); AQSTEP(K + 4); } while (0)
  AQSTEP5(0); AQSTEP5(5); AQSTEP5(10); AQSTEP5(15); AQSTEP5(20); AQSTEP5(25); AQSTEP5(30); AQSTEP5(35);
  bar();
  STAMP(7);
  asm volatile("s_nop 7\n\ts_nop 1" : "+v"(aq[0]), "+v"(aq[1]), "+v"(aq[2]));
  f32x16 pq[3];
  FEATURE_MFMA(aq[0], aq[1], aq[2], pq, INONE);
  STAMP(8);
  const u32x4 sF = mksrd(P.ws + WS_KVF + (G * 4 + w) * 4096, 4096);
  {
    for (unsigned sp = 0; sp < SPIN_CAP; ++sp) {
      HPWAIT();
      bool ok = true;
#pragma unroll
      for (int k = 0; k < 8; ++k) ok &= (hp[k][1] == tagl) & (hp[k][3] == tagh);
      if (__all(ok | (16 * c + i >= 210))) break;
      HPLOAD();
    }
    HPWAIT();
    float sv[4];
#pragma unroll
    for (int k = 0; k < 4; ++k) {
      const int a = k >> 1, e = (k & 1) * 2;
      float v = (__uint_as_float(hp[a][e]) + __uint_as_float(hp[2 + a][e])) + (__uint_as_float(hp[4 + a][e]) + __uint_as_float(hp[6 + a][e]));
      const auto r = __builtin_amdgcn_permlane32_swap(__float_as_uint(v), __float_as_uint(v), false, false);
      const unsigned r0 = r[0], r1 = r[1];
      v = __uint_as_float(r0) + __uint_as_float(r1);
      const auto t = __builtin_amdgcn_permlane16_swap(__float_as_uint(v), __float_as_uint(v), false, false);
      const unsigned t0 = t[0], t1 = t[1];
      sv[k] = __uint_as_float(t0) + __uint_as_float(t1);
    }
    const u32x4 gr = {pk2(sv[0], sv[1]), tagl, pk2(sv[2], sv[3]), tagh};
    __builtin_amdgcn_raw_buffer_store_b128(gr, mkrs(P.ws + WS_KVF + (G * 4 + w) * 4096, 4096), g == 0 ? i * 16 : 0x7fffff00, c * 256, 16);
  }
  STAMP(9);
  u32x4 phq[10], gv[4];
  const unsigned vg = lane * 16;
#define GVLOAD() asm volatile("s_nop 4\n\tbuffer_load_dwordx4 %0, %4, %5, 0 offen sc1\n\tbuffer_load_dwordx4 %1, %4, %5, 0 offen offset:1024 sc1\n\t" \
      "buffer_load_dwordx4 %2, %4, %5, 0 offen offset:2048 sc1\n\tbuffer_load_dwordx4 %3, %4, %5, 0 offen offset:3072 sc1" \
      : "=&v"(gv[0]), "=&v"(gv[1]), "=&v"(gv[2]), "=&v"(gv[3]) : "v"(vg), "s"(sF) : "memory")
#define GVWAIT() asm volatile("s_waitcnt vmcnt(0)" : "+v"(gv[0]), "+v"(gv[1]), "+v"(gv[2]), "+v"(gv[3])::"memory")
#define HOOKA(n) do { if constexpr ((n) % 3 == 0) { SCHED(); AOISSUE(1, (n) / 3); SCHED(); } } while (0)
#define HOOKB(n) do { if constexpr ((n) == 1 || (n) == 4) { SCHED(); AOISSUE(1, (n) == 1 ? 5 : 6); SCHED(); } if constexpr ((n) == 9) { SCHED(); GVLOAD(); SCHED(); } } while (0)
  {
    f32x2 pv[10];
    u32x4 ph[5];
    head_vals<0>(pq, pv); DUAL_SOFTMAX(pv, ph, HOOKA);
#pragma unroll
    for (int u = 0; u < 5; ++u) { phq[u] = ph[u]; asm volatile("" : "+v"(phq[u])); }
    head_vals<1>(pq, pv); DUAL_SOFTMAX(pv, ph, HOOKB);
#pragma unroll
    for (int u = 0; u < 5; ++u) { phq[5 + u] = ph[u]; asm volatile("" : "+v"(phq[5 + u])); }
  }
  unsigned ka[4];
#pragma unroll
  for (int m = 0; m < 4; ++m) {
    const unsigned sl = lane + 64 * m, s1 = sl - 160, q5 = (s1 * 205u) >> 10, f4 = sl < 160 ? sl >> 4 : q5, d = sl < 160 ? sl & 15 : 16 + s1 - 5 * q5;
    ka[m] = A_KVT + w * 1680 + d * 80 + f4 * 8; PIN1(ka[m]);
  }
  unsigned a0, a1, a2, a3;
  {
    const bool val = ((i32 >> 2) & 1) == h;
    const unsigned xo = i32 >> 3, e = i32 & 3, hb = A_KVT + 2 * h * 1680;
    a0 = val ? hb + (e + 4 * xo) * 80 : (unsigned)A_ZERO; a1 = val ? a0 + 1680 : (unsigned)A_ZERO;
    a2 = (val && !(xo & 1)) ? hb + (xo == 0 ? 16 + e : 20) * 80 : (unsigned)A_ZERO;
    a3 = (val && (xo & 1)) ? hb + 1680 + (xo == 1 ? 16 + e : 20) * 80 : (unsigned)A_ZERO;
    asm volatile("" : "+v"(a0), "+v"(a1), "+v"(a2), "+v"(a3));
  }
  STAMP(10);
  for (unsigned sp = 0; sp < SPIN_CAP; ++sp) {
    GVWAIT();
    bool ok = true;
#pragma unroll
    for (int m = 0; m < 4; ++m) ok &= (m == 3 && lane >= 18) | ((gv[m][1] == tagl) & (gv[m][3] == tagh));
    if (__all(ok)) break;
    GVLOAD();
  }
  GVWAIT();
#pragma unroll
  for (int m = 0; m < 3; ++m) *(LAS u32x2*)(lds + ka[m]) = (u32x2){gv[m][0], gv[m][2]};
  if (lane < 18) *(LAS u32x2*)(lds + ka[3]) = (u32x2){gv[3][0], gv[3][2]};
  bar();
  STAMP(11);
  const __amdgpu_buffer_rsrc_t rOb = mkrs(P.ws + WS_OB + c * 163840, 163840);
  const unsigned vol = w * 5120 + lane * 16;
  {
    bf16x8 fa_[4][5];
#pragma unroll
    for (int u = 0; u < 5; ++u) { fa_[2][u] = ldsr128(lds, a2 + 16 * u); fa_[3][u] = ldsr128(lds, a3 + 16 * u); }
#pragma unroll
    for (int u = 0; u < 5; ++u) { fa_[0][u] = ldsr128(lds, a0 + 16 * u); fa_[1][u] = ldsr128(lds, a1 + 16 * u); }
    SCHED();
    f32x16 oo[3];
#pragma unroll
    for (int u = 0; u < 5; ++u) {
      if (u == 0) mfma32z(oo[2], fa_[2][u], __builtin_bit_cast(bf16x8, phq[u])); else mfma32(oo[2], fa_[2][u], __builtin_bit_cast(bf16x8, phq[u]));
      mfma32(oo[2], fa_[3][u], __builtin_bit_cast(bf16x8, phq[5 + u]));
      if (u == 0) AOISSUE(2, 0); if (u == 2) AOISSUE(2, 1); if (u == 4) AOISSUE(2, 2);
    }
#pragma unroll
    for (int u = 0; u < 5; ++u) { if (u == 0) mfma32z(oo[0], fa_[0][u], __builtin_bit_cast(bf16x8, phq[u])); else mfma32(oo[0], fa_[0][u], __builtin_bit_cast(bf16x8, phq[u])); }
    AOISSUE(2, 3);
    mfma32z(oo[1], fa_[1][0], __builtin_bit_cast(bf16x8, phq[5])); mfma32(oo[1], fa_[1][1], __builtin_bit_cast(bf16x8, phq[6]));
    asm volatile("s_nop 4" : "+v"(oo[2]));
    float r0 = __builtin_amdgcn_rcpf(oo[2][8] + 1e-6f), r1 = __builtin_amdgcn_rcpf(oo[2][12] + 1e-6f);
#pragma unroll
    for (int k = 0; k < 4; ++k) { oo[2][k] *= r0; oo[2][4 + k] *= r1; }
    asm volatile("" : "+v"(oo[2]), "+v"(r0), "+v"(r1));
    SCHED();
    mfma32(oo[1], fa_[1][2], __builtin_bit_cast(bf16x8, phq[7])); mfma32(oo[1], fa_[1][3], __builtin_bit_cast(bf16x8, phq[8])); mfma32(oo[1], fa_[1][4], __builtin_bit_cast(bf16x8, phq[9]));
    oo[0] *= r0;
    __builtin_amdgcn_raw_buffer_store_b128(pk8(oo[0], 0), rOb, vol, G * 20480, 16);
    __builtin_amdgcn_raw_buffer_store_b128(pk8(oo[0], 8), rOb, vol, G * 20480 + 1024, 16);
    SCHED();
    asm volatile("s_nop 3" : "+v"(oo[1]));
    oo[1] *= r1;
    __builtin_amdgcn_raw_buffer_store_b128(pk44(oo[2], 0, oo[1], 0), rOb, vol, G * 20480 + 2048, 16);
    __builtin_amdgcn_raw_buffer_store_b128(pk8(oo[1], 4), rOb, vol, G * 20480 + 3072, 16);
    __builtin_amdgcn_raw_buffer_store_b128(pk44(oo[1], 12, oo[2], 4), rOb, vol, G * 20480 + 4096, 16);
  }
  STAMP(12);
  AOISSUE(2, 4); AOISSUE(2, 5); AOISSUE(2, 6);
  LBase<3, 3> aO;
  aO.v[0][0] = (40 * (g >> 1) + 2 * q) * 160 + 32 * (g & 1) + 8 * p;
  aO.v[0][1] = (40 * (g >> 1) + 8 * (g & 1) + 2 * q) * 160 + 128 + 8 * p;
  aO.v[0][2] = (40 * (g >> 1) + 48 * (g & 1) + 2 * q) * 160 + 128 + 8 * p;
  aO.fin();
  bf16x8 fo[2][5];
#define AOOFF(S_) (obase((S_) / 10) + (80 * (((S_) % 10) / 5) + 8 * ((S_) % 5)) * 160)
#define A2RD(M, N) fo[(M) & 1][N] = (N) < 4 ? cat(ldstr(lds, aO.at<AOOFF(2 * (M) + ((N) >> 1 & 1)) + ((N) & 1) * 64>(0)), ldstr(lds, aO.at<AOOFF(2 * (M) + ((N) >> 1 & 1)) + ((N) & 1) * 64 + 160>(0))) \
                                            : cat(ldstr(lds, aO.at<AOOFF(2 * (M))>((2 * (M)) % 5 == 4 ? 2 : 1)), ldstr(lds, aO.at<AOOFF(2 * (M)) + 160>((2 * (M)) % 5 == 4 ? 2 : 1)))
#define A2RDX(M, N) do { if constexpr ((M) < 20) A2RD(M, N); } while (0)
  A2RD(0, 0); A2RD(0, 1); A2RD(0, 2); A2RD(0, 3); A2RD(0, 4);
  vmwait<3>();
  if (lane == 0) __hip_atomic_store((GAS u64*)(P.ws + WS_FA2 + ((c * 4 + w) * 8 + G) * 128), tag, __ATOMIC_RELAXED, __HIP_MEMORY_SCOPE_AGENT);
  STAMP(13);
  AOISSUE(3, 0); AOISSUE(3, 1); AOISSUE(3, 2); AOISSUE(3, 3); AOISSUE(3, 4); AOISSUE(3, 5); AOISSUE(3, 6);
  poll_flags(P.ws + WS_FA2 + (c * 4 + w) * 1024, 128, 8, lane, tag);
  STAMP(14);
  constexpr int OD = 5;
  u32x4 ofr[OD][2];
#define A2LD(M, Q) do { if constexpr ((M) < 20) ofr[(M) % OD][Q] = __builtin_amdgcn_raw_buffer_load_b128(rOb, vol, (((2 * (M) + (Q)) / 5 + 2 * sx) & 7) * 20480 + ((2 * (M) + (Q)) % 5) * 1024, 16); } while (0)
  A2LD(0, 0); A2LD(0, 1); A2LD(1, 0); A2LD(1, 1); A2LD(2, 0); A2LD(2, 1); A2LD(3, 0); A2LD(3, 1);
  if (c == 0 && G == 0 && w == 0 && lane == 0) __hip_atomic_store((GAS u64*)(P.ws + WS_EPOCH_A), tag, __ATOMIC_RELAXED, __HIP_MEMORY_SCOPE_AGENT);
  f32x16 y[2];
  f32x4 z[2] = {(f32x4){0.f, 0.f, 0.f, 0.f}, (f32x4){0.f, 0.f, 0.f, 0.f}};
  SCHED();
#define A2STEP(M) do { if ((M) == 9) bar();   \
    u32x4 f0 = ofr[(M) % OD][0], f1 = ofr[(M) % OD][1]; \
    if ((M) == 0) mfma32z(y[0], __builtin_bit_cast(bf16x8, f0), fo[(M) & 1][0]); else mfma32(y[0], __builtin_bit_cast(bf16x8, f0), fo[(M) & 1][0]); \
    A2RDX((M) + 1, 0); A2RDX((M) + 1, 1); SCHED(); \
    if ((M) == 0) mfma32z(y[1], __builtin_bit_cast(bf16x8, f0), fo[(M) & 1][1]); else mfma32(y[1], __builtin_bit_cast(bf16x8, f0), fo[(M) & 1][1]); \
    A2RDX((M) + 1, 2); A2LD((M) + OD - 1, 0); SCHED(); \
    mfma32(y[0], __builtin_bit_cast(bf16x8, f1), fo[(M) & 1][2]); \
    A2RDX((M) + 1, 3); A2LD((M) + OD - 1, 1); SCHED(); \
    mfma32(y[1], __builtin_bit_cast(bf16x8, f1), fo[(M) & 1][3]); \
    swap16x4(f0, f1); A2RDX((M) + 1, 4); SCHED(); \
    mfma16x2(z[0], z[1], f0, f1, fo[(M) & 1][4]); SCHED(); } while (0)
#define A2STEP4(M) do { A2STEP(M); A2STEP(M + 1); A2STEP(M + 2); A2STEP(M + 3); } while (0)
  A2STEP4(0); A2STEP4(4); A2STEP4(8); A2STEP4(12); A2STEP4(16);
  STAMP(15);
  NOP12("+v"(y[0]), "+v"(y[1]), "+v"(z[0]), "+v"(z[1]));
  {
    const __amdgpu_buffer_rsrc_t rO = mkrs(P.out, 2048 * 5120);
    const unsigned so = (c * 128 + 32 * w) * 5120 + G * 320;
#pragma unroll
    for (int r = 0; r < 16; ++r) {
      const unsigned v = pk2(y[0][r], y[1][r]);
      __builtin_amdgcn_raw_buffer_store_b32(v << 16, rO, h * 20480 + i32 * 4, so + ((r & 3) + 8 * (r >> 2)) * 5120, 16);
      __builtin_amdgcn_raw_buffer_store_b32(v & 0xffff0000u, rO, h * 20480 + 128 + i32 * 4, so + ((r & 3) + 8 * (r >> 2)) * 5120, 16);
    }
#pragma unroll
    for (int r = 0; r < 4; ++r) {
      const unsigned v = pk2(z[0][r], z[1][r]);
      __builtin_amdgcn_raw_buffer_store_b32(v << 16, rO, g * 20480 + 256 + i * 4, so + r * 5120, 16);
      __builtin_amdgcn_raw_buffer_store_b32(v & 0xffff0000u, rO, g * 20480 + 256 + i * 4, so + (16 + r) * 5120, 16);
    }
  }
  LABEL(".Lca_end");
}

typedef unsigned u32x16 __attribute__((ext_vector_type(16)));
typedef unsigned u32x8 __attribute__((ext_vector_type(8)));
template <class T> DI T* mkptr(unsigned lo, unsigned hi) { return (T*)(((u64)hi << 32) | lo); }
__global__ __launch_bounds__(256, 1) void fused_kernel(Params Parg) {
  __shared__ __attribute__((aligned(16))) char smem[163840];
  STAMP(0);
  u32x16 k0; u32x8 k1; u32x4 k2; u32x2 k3;
  asm volatile("s_load_dwordx16 %0, %4, 0x0\n\ts_load_dwordx8 %1, %4, 0x40\n\ts_load_dwordx4 %2, %4, 0x60\n\ts_load_dwordx2 %3, %4, 0x70"
               : "=&s"(k0), "=&s"(k1), "=&s"(k2), "=&s"(k3) : "s"(__builtin_amdgcn_kernarg_segment_ptr()));
  LAS char* lds = (LAS char*)smem;
  const int b = blockIdx.x, xcd = b & 7, loc = b >> 3, sw = loc & 3, sx = (loc >> 2) & 3;
  const int c = (xcd >> 1) * 4 + sw, j = (xcd & 1) * 4 + sx;
  const int rot = ((((xcd >> 1) + 2 * (xcd & 1)) & 3) * 5) >> 1;
  const int w = __builtin_amdgcn_readfirstlane(threadIdx.x >> 6), lane = threadIdx.x & 63;
  int isb = loc >> 4;
  Stage S;
  const int e3 = lane >> 3, pos = lane & 7;
  {
    const unsigned xr = (isb ? 16 * (e3 >> 2) + (e3 & 3) : e3) * 1280;
    const unsigned xe = xr + ((pos ^ (4 * (sx & 1) + (e3 >> 1))) << 4), xs = 10240 >> isb;
#pragma unroll
    for (int u = 0; u < 4; ++u) {
      const int U = (u + sx) & 3;
      S.vx[u] = (xe ^ ((u & 1) * 64)) + U * xs; S.lx[u] = w * 4096 + U * 1024;
    }
    const unsigned vb = e3 * 1280 + ((pos ^ ((e3 & 6) | (w >> 1))) << 4), lb = w * 5120 + (1 - isb) * (w >> 1) * 32;
#pragma unroll
    for (int u = 0; u < 5; ++u) {
      const int U = u + sw >= 5 ? u + sw - 5 : u + sw;
      S.vw[u] = vb + U * 10240; S.lw[u] = lb + U * 1024;
    }
    if (!isb) {
#pragma unroll
      for (int u = 0; u < 5; ++u) {
        const int U = u + sw >= 5 ? u + sw - 5 : u + sw;
        const unsigned Ul = 64 * U + lane, pr = __umul24(Ul, 6554u) >> 16;
        S.vw[u] = __umul24((pr & ~7u) | ((pr & 1u) << 2) | ((pr & 7u) >> 1), 3840) + Ul * 16 - __umul24(pr, 160);
      }
    }
    S.sx0 = (c * 128 + 32 * w) * 1280;
    const unsigned swb = (j * 80 + 40 * (w & 1)) * 1280, swa = (w & 1) * 32 * 3840 + j * 160 + (1 + (w >> 1)) * 1280;
    S.sw0 = swa + isb * (swb - swa);
    S.kmul = 1920 - isb * 1919;
    S.kc = rot * 128;
    asm volatile("" : "+v"(S.vx[0]), "+v"(S.vx[1]), "+v"(S.vx[2]), "+v"(S.vx[3]), "+v"(S.vw[0]), "+v"(S.vw[1]), "+v"(S.vw[2]), "+v"(S.vw[3]), "+v"(S.vw[4]));
    asm volatile("" : "+s"(S.lx[0]), "+s"(S.lx[1]), "+s"(S.lx[2]), "+s"(S.lx[3]), "+s"(S.lw[0]), "+s"(S.lw[1]), "+s"(S.lw[2]), "+s"(S.lw[3]), "+s"(S.lw[4]),
                 "+s"(S.sx0), "+s"(S.sw0), "+s"(S.kmul), "+s"(S.kc));
  }
  SCHED();
  asm volatile("s_waitcnt lgkmcnt(0)" : "+s"(k0), "+s"(k1), "+s"(k2), "+s"(k3));
  Params P;
  P.Ax = mkptr<const u16>(k0[0], k0[1]); P.wqkv = mkptr<const u16>(k0[2], k0[3]); P.wphi = mkptr<const u16>(k0[4], k0[5]); P.wo = mkptr<const u16>(k0[6], k0[7]);
  P.Bx = mkptr<const u16>(k0[8], k0[9]); P.lre = mkptr<const u16>(k0[10], k0[11]); P.lim = mkptr<const u16>(k0[12], k0[13]); P.bre = mkptr<const u16>(k0[14], k0[15]);
  P.bim = mkptr<const u16>(k1[0], k1[1]); P.cre = mkptr<const u16>(k1[2], k1[3]); P.cim = mkptr<const u16>(k1[4], k1[5]); P.logdt = mkptr<const u16>(k1[6], k1[7]);
  P.dsk = mkptr<const u16>(k2[0], k2[1]); P.out = mkptr<float>(k2[2], k2[3]); P.ws = mkptr<char>(k3[0], k3[1]);
  S.sX = mksrd(isb ? P.Bx : P.Ax, 2048 * 1280);
  S.sW = mksrd(isb ? (w >= 2 ? P.bim : P.bre) : P.wqkv, isb ? 640 * 1280 : 640 * 3840);
  g1_all<0, 0>(S);
  {
    u64 pc; unsigned oa, ob, za, zb;
    asm volatile("s_getpc_b64 %0\n.Lpf_here:\n\ts_mov_b32 %1, .Lca_beg-.Lpf_here\n\ts_mov_b32 %2, .Lcb_beg-.Lpf_here\n\ts_mov_b32 %3, .Lca_end-.Lca_beg\n\ts_mov_b32 %4, .Lcb_end-.Lcb_beg"
                 : "=s"(pc), "=s"(oa), "=s"(ob), "=s"(za), "=s"(zb));
    const u32x4 sp = mksrd((const void*)(pc + (isb ? ob : oa)), isb ? zb : za);
    const unsigned vo = lane < 16 ? (sw * 64 + w * 16 + lane) * 128 : 0x7fffff00u;
    asm volatile("s_mov_b32 m0, 0x22300\n\ts_nop 0\n\tbuffer_load_dword %0, %1, 0 offen lds" :: "v"(vo), "s"(sp) : "memory");
  }
  {
    const unsigned vt = lane < 8 ? lane << 21 : 0x7fffff00u, vu = lane < 2 ? (c * 128 + lane * 127) * 5120 : 0x7fffff00u;
    asm volatile("buffer_load_dword %0, %1, 0 offen lds\n\tbuffer_load_dword %2, %3, 0 offen lds" :: "v"(vt), "s"(mksrd(P.ws, WS_END)), "v"(vu), "s"(mksrd(P.out, 2048 * 5120)) : "memory");
  }
  STAMP(1);
  asm volatile("" : "+s"(isb));
  if (isb) chain_b(P, lds, S, c, j, rot, sx, sw, w, lane PROF_PASS);
  else chain_a(P, lds, S, c, j, rot, sx, sw, w, lane PROF_PASS);
  PROF_FLUSH();
}

extern "C" void kernel_launch(void* const* d_in, const int* in_sizes, int n_in, void* d_out, int out_size, void* d_ws,
                              size_t ws_size, hipStream_t stream) {
  Params P;
  P.Ax = (const u16*)d_in[0]; P.wqkv = (const u16*)d_in[1]; P.wphi = (const u16*)d_in[2]; P.wo = (const u16*)d_in[3];
  P.Bx = (const u16*)d_in[4]; P.lre = (const u16*)d_in[5]; P.lim = (const u16*)d_in[6]; P.bre = (const u16*)d_in[7];
  P.bim = (const u16*)d_in[8]; P.cre = (const u16*)d_in[9]; P.cim = (const u16*)d_in[10]; P.logdt = (const u16*)d_in[11];
  P.dsk = (const u16*)d_in[12]; P.out = (float*)d_out; P.ws = (char*)d_ws;
  fused_kernel<<<256, 256, 0, stream>>>(P);
}
```
